```python
import torch

def _softmax(x, axis=-1):
    return torch.softmax(x, dim=axis)
def _gelu(x):
    return torch.nn.functional.gelu(x, approximate="tanh")
def _silu(x):
    return torch.nn.functional.silu(x)
def _sigmoid(x):
    return torch.sigmoid(x)
def _layernorm(x, axis=-1, eps=1e-5):
    m = x.mean(dim=axis, keepdim=True)
    v = x.var(dim=axis, keepdim=True, unbiased=False)
    return (x - m) / torch.sqrt(v + eps)
def _scatter_add(tgt, idx, src, axis=0):
    return tgt.clone().index_add_(axis, idx, src)
def _segment_sum(src, seg, n):
    out = torch.zeros((n,) + src.shape[1:], dtype=src.dtype)
    return out.index_add_(0, seg, src)
def _pad(x, pw):
    flat = tuple(v for pair in reversed(pw) for v in pair)
    return torch.nn.functional.pad(x, flat)

kernel_name = 'gather_scatter__edge_featured_mpnn_mpnn__graph'

def setup_inputs():

    _w = (lambda a: (lambda t: t.to(torch.bfloat16) if t.is_floating_point() else t)(torch.as_tensor(a)))
    N, deg, D, De = 12500, 32, 128, max(4, 1)
    E = N * deg
    H = 2 * D
    return {
        "x": _w((lambda *s: torch.randn(*s, dtype=torch.bfloat16) * 0.05)(N, D)),
        "e_attr": _w((lambda *s: torch.randn(*s, dtype=torch.bfloat16) * 1.6)(E, De)),
        "src": _w((lambda lo, hi, *s: torch.randint(lo, hi, s))(0, N, E).to(torch.int64)),
        "dst": _w((lambda lo, hi, *s: torch.randint(lo, hi, s))(0, N, E).to(torch.int64)),
        "w1": _w((lambda *s: torch.randn(*s, dtype=torch.bfloat16) * 1.6)(2 * D + De, H) * (1.0 / (2 * D + De) ** 0.5)),
        "b1": _w((lambda *s: torch.randn(*s, dtype=torch.bfloat16) * 3.2)(H) * 0.01),
        "w2": _w((lambda *s: torch.randn(*s, dtype=torch.bfloat16) * 0.05)(H, D) * (1.0 / H ** 0.5)),
        "b2": _w((lambda *s: torch.randn(*s, dtype=torch.bfloat16) * 0.1)(D) * 0.01),
    }

def reference(x, e_attr, src, dst, w1, b1, w2, b2):
    N, D = x.shape
    h_src = x[src]
    h_dst = x[dst]
    m_in = torch.concatenate([h_src, h_dst, e_attr], axis=-1)
    h = _silu(m_in @ w1 + b1)
    msg = h @ w2 + b2
    return _segment_sum(msg, dst, N)
```

```cpp
#include <hip/hip_runtime.h>
#include <hip/hip_bf16.h>
#include <stdint.h>

namespace {

constexpr int NN = 12500;
constexpr int NE = 400000;
constexpr int NG = 512;
constexpr int NCH = 256;
constexpr int CH = 1563;
constexpr int CHP = 1568;
constexpr int CAP = 1024;

constexpr size_t WS_P = 0;
constexpr size_t WS_REC = 6400000;
constexpr size_t WS_TAB = WS_REC + (size_t)NCH * CHP * 16;
constexpr size_t WS_WB = WS_TAB + (size_t)NCH * NG * 4;
constexpr size_t WS_W2 = WS_WB + 65536;
constexpr size_t WS_TBL = WS_W2 + 65536;
constexpr size_t WS_EA = WS_TBL + 65536;
constexpr size_t WS_END = WS_EA + (size_t)NG * CAP * 8;

constexpr float LOG2E = 1.4426950408889634f;
constexpr float LN2 = 0.6931471805599453f;
constexpr float LSCALE = 1.0f / 64.0f;

typedef __attribute__((ext_vector_type(8))) short bf16x8;
typedef __attribute__((ext_vector_type(4))) short s16x4;
typedef __attribute__((ext_vector_type(16))) float f32x16;
typedef __attribute__((ext_vector_type(4))) unsigned u32x4;
typedef __attribute__((ext_vector_type(2))) unsigned u32x2;
typedef __bf16 bf16x2_t __attribute__((ext_vector_type(2)));
typedef float f32x2_t __attribute__((ext_vector_type(2)));
#define LDS_AS __attribute__((address_space(3)))
#define DI __device__ __forceinline__

DI unsigned pack_bf16(float a, float b) {
  f32x2_t v = {a, b};
  bf16x2_t r = __builtin_convertvector(v, bf16x2_t);
  return __builtin_bit_cast(unsigned, r);
}
DI float bf2f(unsigned short u) { return __uint_as_float((unsigned)u << 16); }
DI f32x16 mfma(u32x4 a, u32x4 b, f32x16 c) {
  return __builtin_amdgcn_mfma_f32_32x32x16_bf16(__builtin_bit_cast(bf16x8, a), __builtin_bit_cast(bf16x8, b), c, 0, 0, 0);
}
template <typename T> DI T lds_ld(unsigned addr) { return *(const LDS_AS T*)(size_t)addr; }
template <typename T> DI void lds_st(unsigned addr, T v) { *(LDS_AS T*)(size_t)addr = v; }
DI int crow(int i, int h) { return (i & 3) + 8 * (i >> 2) + 4 * h; }
DI int gstart(int g) { return (g * NN + (NG - 1)) / NG; }

constexpr int PB_T1 = 196;
constexpr int PB_T2 = PB_T1 + NCH;
constexpr int PB_WB = PB_T2 + 8;
constexpr int PB_W2 = PB_WB + 8;
constexpr int PB_TBL = PB_W2 + 16;
constexpr int PREP_LDS = 32768 + 128 * 272;

__global__ __launch_bounds__(512) void prep_kernel(const unsigned short* __restrict__ x, const uint32_t* __restrict__ ea,
                                                   const int* __restrict__ src, const int* __restrict__ dst,
                                                   const unsigned short* __restrict__ w1, const unsigned short* __restrict__ w2,
                                                   char* __restrict__ ws) {
  __shared__ __attribute__((aligned(16))) char lds[PREP_LDS];
  const unsigned lb = (unsigned)(size_t)lds;
  const int tid = threadIdx.x, lane = tid & 63, wv = tid >> 6;
  const int blk = blockIdx.x;
  if (blk < PB_T1) {
    const int n0 = (blk >> 1) * 128, half = blk & 1;
    const unsigned XO = lb, WO = lb + 32768;
    u32x4 xv[4], wv4[4];
#pragma unroll
    for (int k = 0; k < 4; ++k) {
      const int q = tid + 512 * k, row = q >> 4, ch = q & 15;
      const int n = min(n0 + row, NN - 1);
      xv[k] = *(const u32x4*)(x + (size_t)n * 128 + ch * 8);
      wv4[k] = *(const u32x4*)(w1 + (size_t)row * 256 + half * 128 + ch * 8);
    }
#pragma unroll
    for (int k = 0; k < 4; ++k) {
      const int q = tid + 512 * k, row = q >> 4, ch = q & 15;
      lds_st<u32x4>(XO + row * 256 + ((ch ^ (row & 15)) << 4), xv[k]);
      lds_st<u32x4>(WO + row * 272 + (ch << 4), wv4[k]);
    }
    __syncthreads();
    const int nt = wv & 3, cp = wv >> 2;
    const int r = lane & 31, h = lane >> 5, bk = (lane >> 4) & 1, q4 = (lane & 15) >> 2, p4 = lane & 3;
    f32x16 acc[2];
#pragma unroll
    for (int i = 0; i < 16; ++i) { acc[0][i] = 0.f; acc[1][i] = 0.f; }
    const int xrow = 32 * nt + r;
#pragma unroll
    for (int ks = 0; ks < 8; ++ks) {
      const u32x4 bfr = lds_ld<u32x4>(XO + xrow * 256 + (((2 * ks + h) ^ (xrow & 15)) << 4));
#pragma unroll
      for (int ct = 0; ct < 2; ++ct) {
        const unsigned a0 = WO + (16 * ks + 8 * h + q4) * 272 + (64 * cp + 32 * ct + 16 * bk + 4 * p4) * 2;
        const s16x4 lo = __builtin_amdgcn_ds_read_tr16_b64_v4i16((LDS_AS s16x4*)(size_t)a0);
        const s16x4 hi = __builtin_amdgcn_ds_read_tr16_b64_v4i16((LDS_AS s16x4*)(size_t)(a0 + 4 * 272));
        const bf16x8 afr = __builtin_shufflevector(lo, hi, 0, 1, 2, 3, 4, 5, 6, 7);
        acc[ct] = mfma(__builtin_bit_cast(u32x4, afr), bfr, acc[ct]);
      }
    }
    __syncthreads();
#pragma unroll
    for (int ct = 0; ct < 2; ++ct) {
      const float sc = ct ? LOG2E : LSCALE;
#pragma unroll
      for (int g = 0; g < 4; ++g) {
        u32x2 pk;
        pk[0] = pack_bf16(acc[ct][4 * g] * sc, acc[ct][4 * g + 1] * sc);
        pk[1] = pack_bf16(acc[ct][4 * g + 2] * sc, acc[ct][4 * g + 3] * sc);
        lds_st<u32x2>(WO + xrow * 272 + (64 * cp + 32 * ct + 8 * g + 4 * h) * 2, pk);
      }
    }
    __syncthreads();
#pragma unroll
    for (int k = 0; k < 4; ++k) {
      const int q = tid + 512 * k, row = q >> 4, ch = q & 15;
      if (n0 + row < NN) *(u32x4*)(ws + WS_P + (size_t)(n0 + row) * 512 + half * 256 + ch * 16) = lds_ld<u32x4>(WO + row * 272 + (ch << 4));
    }
  } else if (blk < PB_T2) {
    const int b = blk - PB_T1;
    const unsigned HO = lb, SO = lb + 2048;
    lds_st<unsigned>(HO + tid * 4, 0u);
    int dv[4], sv[4];
    u32x2 ev[4];
#pragma unroll
    for (int k = 0; k < 4; ++k) {
      const int i = tid + 512 * k, e = b * CH + i;
      const bool ok = i < CH && e < NE;
      const int ec = ok ? e : 0;
      dv[k] = ok ? dst[ec] : -1;
      sv[k] = src[ec];
      ev[k] = *(const u32x2*)(ea + (size_t)ec * 2);
    }
    __syncthreads();
    int gk[4];
    unsigned rk[4];
#pragma unroll
    for (int k = 0; k < 4; ++k) {
      gk[k] = dv[k] >= 0 ? (int)(((unsigned)dv[k] * NG) / NN) : 0;
      rk[k] = 0;
      if (dv[k] >= 0) rk[k] = __hip_atomic_fetch_add((LDS_AS unsigned*)(size_t)(HO + gk[k] * 4), 1u, __ATOMIC_RELAXED, __HIP_MEMORY_SCOPE_WORKGROUP);
    }
    __syncthreads();
    const unsigned c = lds_ld<unsigned>(HO + tid * 4);
    unsigned inc = c;
#pragma unroll
    for (int d = 1; d < 64; d <<= 1) {
      const unsigned t = __shfl_up(inc, d, 64);
      if (lane >= d) inc += t;
    }
    if (lane == 63) lds_st<unsigned>(SO + wv * 4, inc);
    __syncthreads();
    unsigned pre = 0;
#pragma unroll
    for (int w = 0; w < 8; ++w) {
      const unsigned t = lds_ld<unsigned>(SO + w * 4);
      if (w < wv) pre += t;
    }
    const unsigned start = pre + inc - c;
    ((uint32_t*)(ws + WS_TAB))[b * NG + tid] = start | (c << 16);
    lds_st<unsigned>(HO + tid * 4, start);
    __syncthreads();
#pragma unroll
    for (int k = 0; k < 4; ++k) {
      if (dv[k] >= 0) {
        const unsigned st = lds_ld<unsigned>(HO + gk[k] * 4);
        u32x4 rec;
        rec[0] = ((unsigned)sv[k] << 9) | ((unsigned)(dv[k] - gstart(gk[k])) << 4);
        rec[1] = ev[k][0];
        rec[2] = ev[k][1];
        rec[3] = 0;
        *(u32x4*)(ws + WS_REC + ((size_t)b * CHP + st + rk[k]) * 16) = rec;
      }
    }
  } else if (blk < PB_WB) {
    const int q = (blk - PB_T2) * 512 + tid;
    const int l = q & 63, ks = (q >> 6) & 7, ct = q >> 9;
    const int col = 32 * ct + (l & 31), k0 = 128 + 16 * ks + 8 * (l >> 5);
    unsigned short v[8];
#pragma unroll
    for (int j = 0; j < 8; ++j) v[j] = w1[(size_t)(k0 + j) * 256 + col];
    u32x4 o;
#pragma unroll
    for (int j = 0; j < 4; ++j) o[j] = v[2 * j] | ((unsigned)v[2 * j + 1] << 16);
    *(u32x4*)(ws + WS_WB + (size_t)q * 16) = o;
  } else if (blk < PB_W2) {
    const int q = (blk - PB_WB) * 512 + tid;
    const int l = q & 63, ot = (q >> 6) & 3, ks = q >> 8;
    const int col = 32 * ot + (l & 31), h = l >> 5;
    unsigned short v[8];
#pragma unroll
    for (int j = 0; j < 8; ++j) v[j] = w2[(size_t)(32 * (ks >> 1) + crow(8 * (ks & 1) + j, h)) * 128 + col];
    u32x4 o;
#pragma unroll
    for (int j = 0; j < 4; ++j) o[j] = v[2 * j] | ((unsigned)v[2 * j + 1] << 16);
    *(u32x4*)(ws + WS_W2 + (size_t)q * 16) = o;
  } else {
    const int q = (blk - PB_W2) * 512 + tid;
    float hv[4];
#pragma unroll
    for (int j = 0; j < 4; ++j) {
      const int i = 4 * q + j;
      const unsigned code = i < 16384 ? (unsigned)i : 0x8000u + (unsigned)(i - 16384);
      const float z = __uint_as_float(code << 16) * 64.0f;
      hv[j] = z / (1.0f + expf(-z));
    }
    u32x2 o;
    o[0] = pack_bf16(hv[0], hv[1]);
    o[1] = pack_bf16(hv[2], hv[3]);
    *(u32x2*)(ws + WS_TBL + (size_t)q * 8) = o;
  }
}

constexpr unsigned L_TP = 0;
constexpr unsigned L_WORD = 32768;
constexpr unsigned WORD_TEAM = 36 * 128;
constexpr unsigned L_MASK = L_WORD + 2 * WORD_TEAM;
constexpr unsigned MASK_TEAM = 34 * 128;
constexpr unsigned L_ALUT = L_MASK + 2 * MASK_TEAM;
constexpr unsigned L_BLUT = L_ALUT + 2048;
constexpr unsigned L_EA = L_BLUT + 4096;
constexpr unsigned L_CNT = L_EA + 4096;
constexpr unsigned L_TOT = L_CNT + 256;
constexpr unsigned L_TN = 65536;
constexpr unsigned L_STG = 98304;
constexpr unsigned L_END = 163840;
static_assert(L_TOT + 8 <= 65536, "LDS region A overflow");
constexpr unsigned PADWORD = 0x7FFFFE00u | (31u << 4);

struct TileRegs {
  u32x4 p[4];
  u32x4 a[2];
  u32x4 b[2];
};

__global__ __launch_bounds__(512, 2) void main_kernel(const unsigned short* __restrict__ x, const unsigned short* __restrict__ w1,
                                                      const unsigned short* __restrict__ b1, const unsigned short* __restrict__ b2,
                                                      char* __restrict__ ws, unsigned short* __restrict__ out) {
  __shared__ __attribute__((aligned(16))) char lds[L_END];
  asm volatile("" ::"s"((unsigned)(size_t)lds));
  const int tid = threadIdx.x, lane = tid & 63;
  const int wv = __builtin_amdgcn_readfirstlane(tid >> 6);
  const int team = wv >> 2, w = wv & 3, tt = tid & 255;
  const int wg = (blockIdx.x & 7) * 32 + (blockIdx.x >> 3);
  const int g = 2 * wg + team;
  const int n0 = gstart(g), G = gstart(g + 1) - n0;
  const int r = lane & 31, h = lane >> 5;

  const uint32_t tabv = ((const uint32_t*)(ws + WS_TAB))[tt * NG + g];
  u32x4 xs[2];
#pragma unroll
  for (int k = 0; k < 2; ++k) {
    const int q = tt + 256 * k, row = q >> 4, ch = q & 15;
    const int n = min(n0 + row, NN - 1);
    xs[k] = *(const u32x4*)(x + (size_t)n * 128 + ch * 8);
  }
  u32x4 wb[16];
#pragma unroll
  for (int k = 0; k < 16; ++k) wb[k] = *(const u32x4*)(ws + WS_WB + (size_t)((2 * w) * 8 + k) * 1024 + lane * 16);
  float cin[2][3];
#pragma unroll
  for (int t = 0; t < 2; ++t) {
    const int col = 64 * w + 32 * t + r;
    cin[t][0] = bf2f(b1[col]);
    cin[t][1] = bf2f(w1[(size_t)(256 + 2 * h) * 256 + col]);
    cin[t][2] = bf2f(w1[(size_t)(257 + 2 * h) * 256 + col]);
  }
  u32x4 tb[8];
#pragma unroll
  for (int k = 0; k < 8; ++k) tb[k] = *(const u32x4*)(ws + WS_TBL + (size_t)(tid + 512 * k) * 16);

  for (int i = tt; i < 36 * 32; i += 256) lds_st<unsigned>(L_WORD + team * WORD_TEAM + i * 4, PADWORD);
  for (int i = tt; i < 34 * 32; i += 256) lds_st<unsigned>(L_MASK + team * MASK_TEAM + i * 4, 0u);
  if (tt < 32) lds_st<unsigned>(L_CNT + team * 128 + tt * 4, 0u);
  if (tt == 32) lds_st<unsigned>(L_TOT + team * 4, 0u);
  if (tid < 128) {
    const int u = tid & 31, hh = (tid >> 5) & 1, s = tid >> 6;
    const int rho = u < 26 ? u : u + 2;
    u32x4 v = {0, 0, 0, 0};
    if (u < 28) {
#pragma unroll
      for (int j = 0; j < 8; ++j)
        if (crow(8 * s + j, hh) == rho) v[j >> 1] = 0x3F80u << (16 * (j & 1));
    }
    lds_st<u32x4>(L_ALUT + tid * 16, v);
  } else if (tid < 384) {
    const int bb = tid - 128;
    u32x4 v;
#pragma unroll
    for (int j = 0; j < 4; ++j) v[j] = ((bb >> (2 * j)) & 1 ? 0x3F80u : 0u) | ((bb >> (2 * j + 1)) & 1 ? 0x3F800000u : 0u);
    lds_st<u32x4>(L_BLUT + bb * 16, v);
  }
  const unsigned XO = L_STG + team * 32768;
#pragma unroll
  for (int k = 0; k < 2; ++k) {
    const int q = tt + 256 * k, row = q >> 4, ch = q & 15;
    u32x4 v = xs[k];
    if (row >= G) v = u32x4{0, 0, 0, 0};
    lds_st<u32x4>(XO + row * 256 + ((ch ^ (row & 15)) << 4), v);
  }
  __syncthreads();

  const unsigned segs = tabv & 0xFFFFu, segc = tabv >> 16;
  const unsigned pos0 = __hip_atomic_fetch_add((LDS_AS unsigned*)(size_t)(L_TOT + team * 4), segc, __ATOMIC_RELAXED, __HIP_MEMORY_SCOPE_WORKGROUP);
  const char* recp = ws + WS_REC + ((size_t)tt * CHP + segs) * 16;
  char* eap = ws + WS_EA + (size_t)g * CAP * 8;

  u32x4 qf[2][2];
  {
    f32x16 acc[2];
#pragma unroll
    for (int t = 0; t < 2; ++t) {
#pragma unroll
      for (int i = 0; i < 14; ++i) acc[t][i] = cin[t][0];
      acc[t][14] = cin[t][1];
      acc[t][15] = cin[t][2];
    }
    const int u = r < 26 ? r : (r == 28 || r == 29) ? r - 2 : 31;
#pragma unroll
    for (int ks = 0; ks < 8; ++ks) {
      const u32x4 af = lds_ld<u32x4>(XO + u * 256 + (((2 * ks + h) ^ (u & 15)) << 4));
      acc[0] = mfma(af, wb[ks], acc[0]);
      acc[1] = mfma(af, wb[8 + ks], acc[1]);
    }
#pragma unroll
    for (int t = 0; t < 2; ++t) {
      const float sc = t ? LOG2E : LSCALE;
#pragma unroll
      for (int s = 0; s < 2; ++s)
#pragma unroll
        for (int j = 0; j < 4; ++j) qf[t][s][j] = pack_bf16(acc[t][8 * s + 2 * j] * sc, acc[t][8 * s + 2 * j + 1] * sc);
    }
  }
#pragma unroll
  for (int k = 0; k < 8; ++k) {
    const unsigned o = (unsigned)(tid + 512 * k) * 16;
    lds_st<u32x4>(o < 32768 ? o : o + 32768, tb[k]);
  }

  u32x4 idn[2];
#pragma unroll
  for (int s = 0; s < 2; ++s)
#pragma unroll
    for (int j = 0; j < 4; ++j) {
      const int k0 = 16 * s + 8 * h + 2 * j;
      idn[s][j] = (r == k0 ? 0x3F80u : 0u) | (r == k0 + 1 ? 0x3F800000u : 0u);
    }
  f32x16 S[2];
#pragma unroll
  for (int i = 0; i < 16; ++i) { S[0][i] = 0.f; S[1][i] = 0.f; }

  const unsigned lc0 = (unsigned)(w * 128 + (((lane & 7) ^ ((lane >> 4) & 3)) << 4));
  const unsigned lc1 = lc0 ^ 64u;
  const unsigned stg = L_STG + wv * 8192;
  const unsigned pb = (unsigned)(r * 128 + ((h ^ ((r >> 1) & 7)) << 4));
  const unsigned pa0 = stg + pb, pa1 = stg + (pb ^ 32u), pa2 = stg + (pb ^ 64u), pa3 = stg + (pb ^ 96u);
  const unsigned w4a = L_WORD + team * WORD_TEAM + (lane >> 3) * 16;
  const unsigned w1a = L_WORD + team * WORD_TEAM + ((r & 7) * 4 + (r >> 3)) * 4;
  const unsigned mka = L_MASK + team * MASK_TEAM + r * 4 + h;
  const unsigned eal = L_EA + wv * 512 + lane * 4;
  const unsigned eav = (unsigned)((2 * r + h) * 4);
  const unsigned alh = L_ALUT + h * 512;
  const __amdgpu_buffer_rsrc_t srdP = __builtin_amdgcn_make_buffer_rsrc((void*)(ws + WS_P), (short)0, NN * 512, 0x00020000);

  unsigned total = 0;
  int nrounds = 1;
  for (int round = 0; round < nrounds; ++round) {
    const unsigned lo = (unsigned)round * CAP;
    if (round > 0) {
      __syncthreads();
      for (int i = tt; i < 36 * 32; i += 256) lds_st<unsigned>(L_WORD + team * WORD_TEAM + i * 4, PADWORD);
      for (int i = tt; i < 34 * 32; i += 256) lds_st<unsigned>(L_MASK + team * MASK_TEAM + i * 4, 0u);
      __syncthreads();
    }
    for (unsigned k0 = 0; k0 < segc; k0 += 4) {
      u32x4 rec[4];
#pragma unroll
      for (int k = 0; k < 4; ++k) rec[k] = *(const u32x4*)(recp + (size_t)min(k0 + k, segc - 1) * 16);
#pragma unroll
      for (int k = 0; k < 4; ++k) {
        const unsigned p = pos0 + k0 + k - lo;
        if (k0 + k < segc && p < (unsigned)CAP) {
          const unsigned tile = p >> 5, rr = p & 31;
          lds_st<unsigned>(L_WORD + team * WORD_TEAM + tile * 128 + ((rr & 7) * 4 + (rr >> 3)) * 4, rec[k][0]);
          const unsigned u = (rec[k][0] >> 4) & 31;
          const unsigned bit = 16 * (rr >> 4) + 8 * ((rr >> 2) & 1) + 4 * ((rr >> 3) & 1) + (rr & 3);
          __hip_atomic_fetch_or((LDS_AS unsigned*)(size_t)(L_MASK + team * MASK_TEAM + tile * 128 + u * 4), 1u << bit, __ATOMIC_RELAXED, __HIP_MEMORY_SCOPE_WORKGROUP);
          __hip_atomic_fetch_add((LDS_AS unsigned*)(size_t)(L_CNT + team * 128 + u * 4), 1u, __ATOMIC_RELAXED, __HIP_MEMORY_SCOPE_WORKGROUP);
          *(u32x2*)(eap + (size_t)p * 8) = u32x2{rec[k][1], rec[k][2]};
        }
      }
    }
    asm volatile("s_waitcnt vmcnt(0)" ::: "memory");
    __syncthreads();
    if (round == 0) {
      const unsigned t0 = lds_ld<unsigned>(L_TOT), t1 = lds_ld<unsigned>(L_TOT + 4);
      total = __builtin_amdgcn_readfirstlane(team ? t1 : t0);
      nrounds = __builtin_amdgcn_readfirstlane((int)((max(t0, t1) + CAP - 1) / CAP));
      if (nrounds < 1) nrounds = 1;
    }
    const unsigned rem = total > lo ? min(total - lo, (unsigned)CAP) : 0u;
    const int ntiles = (int)((rem + 31) >> 5);
    const __amdgpu_buffer_rsrc_t srdE = __builtin_amdgcn_make_buffer_rsrc((void*)eap, (short)0, (int)(rem * 8), 0x00020000);

    auto dma = [&](int T, int B, u32x4 wd) {
      const unsigned v0 = (wd[0] & 0xFFFFFE00u) | lc0, v1 = (wd[1] & 0xFFFFFE00u) | lc1;
      const unsigned v2 = (wd[2] & 0xFFFFFE00u) | lc0, v3 = (wd[3] & 0xFFFFFE00u) | lc1;
      const unsigned ve = eav + (unsigned)T * 256u;
      const unsigned m0p = stg + B * 4096, m0e = L_EA + wv * 512 + B * 256;
      asm volatile(
          "s_mov_b32 m0, %5\n\ts_nop 0\n\tbuffer_load_dwordx4 %0, %7, 0 offen lds\n\t"
          "s_add_u32 m0, m0, 0x400\n\ts_nop 0\n\tbuffer_load_dwordx4 %1, %7, 0 offen lds\n\t"
          "s_add_u32 m0, m0, 0x400\n\ts_nop 0\n\tbuffer_load_dwordx4 %2, %7, 0 offen lds\n\t"
          "s_add_u32 m0, m0, 0x400\n\ts_nop 0\n\tbuffer_load_dwordx4 %3, %7, 0 offen lds\n\t"
          "s_mov_b32 m0, %6\n\ts_nop 0\n\tbuffer_load_dword %4, %8, 0 offen lds"
          ::"v"(v0), "v"(v1), "v"(v2), "v"(v3), "v"(ve), "s"(m0p), "s"(m0e), "s"(srdP), "s"(srdE)
          : "memory", "scc");
    };
    auto load_tile = [&](int T, int B) {
      TileRegs R;
      R.p[0] = lds_ld<u32x4>(pa0 + B * 4096);
      R.p[1] = lds_ld<u32x4>(pa1 + B * 4096);
      R.p[2] = lds_ld<u32x4>(pa2 + B * 4096);
      R.p[3] = lds_ld<u32x4>(pa3 + B * 4096);
      const unsigned e = lds_ld<unsigned>(eal + B * 256);
      const unsigned wd = lds_ld<unsigned>(w1a + T * 128);
      const unsigned m0 = lds_ld<unsigned char>(mka + T * 128), m1 = lds_ld<unsigned char>(mka + T * 128 + 2);
      const unsigned aa = (wd & 0x1F0u) | alh;
      R.a[0] = lds_ld<u32x4>(aa);
      R.a[1] = lds_ld<u32x4>(aa + 1024);
      R.a[1][3] = e;
      R.b[0] = lds_ld<u32x4>(L_BLUT + m0 * 16);
      R.b[1] = lds_ld<u32x4>(L_BLUT + m1 * 16);
      return R;
    };
    auto compute = [&](const TileRegs& R) {
      f32x16 z0, z1;
#pragma unroll
      for (int i = 0; i < 16; ++i) { z0[i] = 0.f; z1[i] = 0.f; }
      z0 = mfma(R.a[0], qf[0][0], z0);
      z1 = mfma(R.a[0], qf[1][0], z1);
      z0 = mfma(R.a[1], qf[0][1], z0);
      z1 = mfma(R.a[1], qf[1][1], z1);
      z0 = mfma(R.p[0], idn[0], z0);
      z1 = mfma(R.p[2], idn[0], z1);
      z0 = mfma(R.p[1], idn[1], z0);
      z1 = mfma(R.p[3], idn[1], z1);
      unsigned cd[8];
      unsigned any = 0;
#pragma unroll
      for (int j = 0; j < 8; ++j) { cd[j] = pack_bf16(z0[2 * j], z0[2 * j + 1]); any |= cd[j]; }
      u32x4 h0[2];
      if (__builtin_expect(__builtin_amdgcn_ballot_w64((any & 0x40004000u) != 0) != 0, 0)) {
#pragma unroll
        for (int j = 0; j < 8; ++j) {
          const float za = z0[2 * j] * 64.0f, zb = z0[2 * j + 1] * 64.0f;
          const float ha = za * __builtin_amdgcn_rcpf(1.0f + __builtin_amdgcn_exp2f(-LOG2E * za));
          const float hb = zb * __builtin_amdgcn_rcpf(1.0f + __builtin_amdgcn_exp2f(-LOG2E * zb));
          h0[j >> 2][j & 3] = pack_bf16(ha, hb);
        }
      } else {
#pragma unroll
        for (int j = 0; j < 8; ++j) {
          unsigned alo;
          asm("v_lshlrev_b32_sdwa %0, %1, %2 dst_sel:DWORD dst_unused:UNUSED_PAD src0_sel:DWORD src1_sel:WORD_0" : "=v"(alo) : "v"(1u), "v"(cd[j]));
          const unsigned elo = lds_ld<unsigned short>(alo), ehi = lds_ld<unsigned short>((cd[j] >> 16) << 1);
          h0[j >> 2][j & 3] = elo | (ehi << 16);
        }
      }
      u32x4 h1[2];
#pragma unroll
      for (int j = 0; j < 8; ++j) {
        const float za = z1[2 * j], zb = z1[2 * j + 1];
        const float ha = za * __builtin_amdgcn_rcpf(1.0f + __builtin_amdgcn_exp2f(-za));
        const float hb = zb * __builtin_amdgcn_rcpf(1.0f + __builtin_amdgcn_exp2f(-zb));
        h1[j >> 2][j & 3] = pack_bf16(ha, hb);
      }
      S[1] = mfma(h1[0], R.b[0], S[1]);
      S[1] = mfma(h1[1], R.b[1], S[1]);
      S[0] = mfma(h0[0], R.b[0], S[0]);
      S[0] = mfma(h0[1], R.b[1], S[0]);
    };

    if (ntiles > 0) {
      dma(0, 0, lds_ld<u32x4>(w4a));
      dma(1, 1, lds_ld<u32x4>(w4a + 128));
      asm volatile("s_waitcnt vmcnt(5)" ::: "memory");
      TileRegs cur = load_tile(0, 0);
      {
        const u32x4 wd = lds_ld<u32x4>(w4a + 256);
        asm volatile("" ::"v"(cur.p[0]), "v"(cur.p[1]), "v"(cur.p[2]), "v"(cur.p[3]), "v"(cur.a[1]));
        dma(2, 0, wd);
      }
      int t = 0;
      for (; t + 1 < ntiles; t += 2) {
        {
          asm volatile("s_waitcnt vmcnt(5)" ::: "memory");
          TileRegs nxt = load_tile(t + 1, 1);
          const u32x4 wd = lds_ld<u32x4>(w4a + (t + 3) * 128);
          compute(cur);
          asm volatile("" ::"v"(nxt.p[0]), "v"(nxt.p[1]), "v"(nxt.p[2]), "v"(nxt.p[3]), "v"(nxt.a[1]));
          dma(t + 3, 1, wd);
          cur = nxt;
        }
        {
          asm volatile("s_waitcnt vmcnt(5)" ::: "memory");
          TileRegs nxt = load_tile(t + 2, 0);
          const u32x4 wd = lds_ld<u32x4>(w4a + (t + 4) * 128);
          compute(cur);
          asm volatile("" ::"v"(nxt.p[0]), "v"(nxt.p[1]), "v"(nxt.p[2]), "v"(nxt.p[3]), "v"(nxt.a[1]));
          dma(t + 4, 0, wd);
          cur = nxt;
        }
      }
      if (t < ntiles) compute(cur);
      asm volatile("s_waitcnt vmcnt(0)" ::: "memory");
    }
  }

  u32x4 w2f[16];
#pragma unroll
  for (int ks = 0; ks < 16; ++ks) w2f[ks] = *(const u32x4*)(ws + WS_W2 + (size_t)(ks * 4 + w) * 1024 + lane * 16);
  u32x2 b2v[4];
#pragma unroll
  for (int gq = 0; gq < 4; ++gq) b2v[gq] = *(const u32x2*)(b2 + 32 * w + 8 * gq + 4 * h);
  __syncthreads();
  const unsigned EX = L_STG + team * 32768;
#pragma unroll
  for (int t = 0; t < 2; ++t) {
    const float sc = t ? LN2 : 1.0f;
#pragma unroll
    for (int s = 0; s < 2; ++s) {
      u32x4 v;
#pragma unroll
      for (int j = 0; j < 4; ++j) v[j] = pack_bf16(S[t][8 * s + 2 * j] * sc, S[t][8 * s + 2 * j + 1] * sc);
      lds_st<u32x4>(EX + (4 * w + 2 * t + s) * 1024 + lane * 16, v);
    }
  }
  __syncthreads();
  const float deg = (float)lds_ld<unsigned>(L_CNT + team * 128 + r * 4);
  f32x16 o;
#pragma unroll
  for (int gq = 0; gq < 4; ++gq) {
    o[4 * gq + 0] = deg * __uint_as_float(b2v[gq][0] << 16);
    o[4 * gq + 1] = deg * __uint_as_float(b2v[gq][0] & 0xFFFF0000u);
    o[4 * gq + 2] = deg * __uint_as_float(b2v[gq][1] << 16);
    o[4 * gq + 3] = deg * __uint_as_float(b2v[gq][1] & 0xFFFF0000u);
  }
#pragma unroll
  for (int ks = 0; ks < 16; ++ks) o = mfma(w2f[ks], lds_ld<u32x4>(EX + ks * 1024 + lane * 16), o);
  if (r < G) {
    unsigned short* op = out + (size_t)(n0 + r) * 128 + 32 * w + 4 * h;
#pragma unroll
    for (int gq = 0; gq < 4; ++gq) {
      u32x2 v;
      v[0] = pack_bf16(o[4 * gq], o[4 * gq + 1]);
      v[1] = pack_bf16(o[4 * gq + 2], o[4 * gq + 3]);
      *(u32x2*)(op + 8 * gq) = v;
    }
  }
}

}

extern "C" void kernel_launch(void* const* d_in, const int* in_sizes, int n_in, void* d_out, int out_size, void* d_ws,
                              size_t ws_size, hipStream_t stream) {
  const unsigned short* x = (const unsigned short*)d_in[0];
  const uint32_t* ea = (const uint32_t*)d_in[1];
  const int* src = (const int*)d_in[2];
  const int* dst = (const int*)d_in[3];
  const unsigned short* w1 = (const unsigned short*)d_in[4];
  const unsigned short* b1 = (const unsigned short*)d_in[5];
  const unsigned short* w2 = (const unsigned short*)d_in[6];
  const unsigned short* b2 = (const unsigned short*)d_in[7];
  (void)in_sizes; (void)n_in; (void)out_size; (void)ws_size;
  prep_kernel<<<PB_TBL, 512, 0, stream>>>(x, ea, src, dst, w1, w2, (char*)d_ws);
  main_kernel<<<256, 512, 0, stream>>>(x, w1, b1, b2, (char*)d_ws, (unsigned short*)d_out);
}
```

```cpp
#include <hip/hip_runtime.h>
#include <hip/hip_bf16.h>
#include <stdint.h>
#include <type_traits>

#define ABL 0
#define MAIN_GRID 256
#define STORE_AUX 16
#define PREF 1
#define PREF_LINES 196
#define PRIO 1
namespace {

constexpr int NN = 12500;
constexpr int NE = 400000;
constexpr int NG = 512;
constexpr int NCH = 256;
constexpr int CH = 1563;
constexpr int CHP = 1568;
constexpr int CAP = 1024;

constexpr size_t WS_P = 4096;
constexpr size_t WS_REC = WS_P + 6400000;
constexpr size_t WS_TAB = WS_REC + (size_t)NCH * CHP * 12;
constexpr size_t WS_WB = WS_TAB + (size_t)NCH * NG * 4;
constexpr size_t WS_W2 = WS_WB + 65536;
constexpr size_t WS_MX = WS_W2 + 65536;
constexpr size_t WS_EA = WS_MX + 2048;
constexpr size_t WS_END = WS_EA + (size_t)NG * CAP * 8;
#define STAMP(k) do { } while (0)
#define CSTAMP(k) do { } while (0)
#define PSTAMP(k) do { } while (0)

constexpr float LOG2E = 1.4426950408889634f;
constexpr float LN2 = 0.6931471805599453f;
constexpr float LSCALE = 1.0f / 64.0f;

typedef __attribute__((ext_vector_type(8))) short bf16x8;
typedef __attribute__((ext_vector_type(4))) short s16x4;
typedef __attribute__((ext_vector_type(16))) float f32x16;
typedef __attribute__((ext_vector_type(4))) unsigned u32x4;
typedef __attribute__((ext_vector_type(2))) unsigned u32x2;
typedef __attribute__((ext_vector_type(3))) unsigned u32x3;
typedef __attribute__((ext_vector_type(3), aligned(4))) unsigned u32x3_a4;
typedef __attribute__((ext_vector_type(2))) unsigned short u16x2;
typedef __bf16 bf16x2_t __attribute__((ext_vector_type(2)));
typedef float f32x2_t __attribute__((ext_vector_type(2)));
#define LDS_AS __attribute__((address_space(3)))
#define DI __device__ __forceinline__

DI unsigned pack_bf16(float a, float b) {
  f32x2_t v = {a, b};
  bf16x2_t r = __builtin_convertvector(v, bf16x2_t);
  return __builtin_bit_cast(unsigned, r);
}
DI float bf2f(unsigned short u) { return __uint_as_float((unsigned)u << 16); }
DI f32x16 mfma(u32x4 a, u32x4 b, f32x16 c) {
  return __builtin_amdgcn_mfma_f32_32x32x16_bf16(__builtin_bit_cast(bf16x8, a), __builtin_bit_cast(bf16x8, b), c, 0, 0, 0);
}
template <typename T> DI T lds_ld(unsigned addr) { return *(const LDS_AS T*)(size_t)addr; }
template <typename T> DI void lds_st(unsigned addr, T v) { *(LDS_AS T*)(size_t)addr = v; }
DI int crow(int i, int h) { return (i & 3) + 8 * (i >> 2) + 4 * h; }
DI unsigned wave_incl_scan(unsigned v) {
  v += __builtin_amdgcn_update_dpp(0u, v, 0x111, 0xF, 0xF, false);
  v += __builtin_amdgcn_update_dpp(0u, v, 0x112, 0xF, 0xF, false);
  v += __builtin_amdgcn_update_dpp(0u, v, 0x114, 0xF, 0xF, false);
  v += __builtin_amdgcn_update_dpp(0u, v, 0x118, 0xF, 0xF, false);
  v += __builtin_amdgcn_update_dpp(0u, v, 0x142, 0xA, 0xF, false);
  v += __builtin_amdgcn_update_dpp(0u, v, 0x143, 0xC, 0xF, false);
  return v;
}
DI unsigned wave_max_u32(unsigned v) {
  v = max(v, (unsigned)__builtin_amdgcn_update_dpp(0u, v, 0x111, 0xF, 0xF, false));
  v = max(v, (unsigned)__builtin_amdgcn_update_dpp(0u, v, 0x112, 0xF, 0xF, false));
  v = max(v, (unsigned)__builtin_amdgcn_update_dpp(0u, v, 0x114, 0xF, 0xF, false));
  v = max(v, (unsigned)__builtin_amdgcn_update_dpp(0u, v, 0x118, 0xF, 0xF, false));
  v = max(v, (unsigned)__builtin_amdgcn_update_dpp(0u, v, 0x142, 0xA, 0xF, false));
  v = max(v, (unsigned)__builtin_amdgcn_update_dpp(0u, v, 0x143, 0xC, 0xF, false));
  return v;
}
DI unsigned absbits(float f) { return __float_as_uint(f) & 0x7FFFFFFFu; }
DI void lds_max(unsigned addr, unsigned v) {
  __hip_atomic_fetch_max((LDS_AS unsigned*)(size_t)addr, v, __ATOMIC_RELAXED, __HIP_MEMORY_SCOPE_WORKGROUP);
}
DI int gstart(int g) { return (g * NN + (NG - 1)) / NG; }

constexpr int PB_T1 = 196;
constexpr int PB_T2 = PB_T1 + NCH;
constexpr int PB_WB = PB_T2 + 8;
constexpr int PB_W2 = PB_WB + 8;
constexpr int PREP_LDS = 65536 + 64;

__global__ __launch_bounds__(512) void prep_kernel(const unsigned short* __restrict__ x, const uint32_t* __restrict__ ea,
                                                   const int* __restrict__ src, const int* __restrict__ dst,
                                                   const unsigned short* __restrict__ w1, const unsigned short* __restrict__ w2,
                                                   char* __restrict__ ws) {
  __shared__ __attribute__((aligned(16))) char lds[PREP_LDS];
  const unsigned lb = (unsigned)(size_t)lds;
  const int tid = threadIdx.x, lane = tid & 63, wv = tid >> 6;
  const int blk = blockIdx.x;
  PSTAMP(0);
  if (blk < PB_T1) {
    const int n0 = (blk >> 1) * 128, half = blk & 1;
    const unsigned XO = lb, WO = lb + 32768;
    const __amdgpu_buffer_rsrc_t srdPw = __builtin_amdgcn_make_buffer_rsrc((void*)(ws + WS_P), (short)0, NN * 512, 0x00020000);
    u32x4 xv[4], wv4[4];
#pragma unroll
    for (int k = 0; k < 4; ++k) {
      const int q = tid + 512 * k, row = q >> 4, ch = q & 15;
      const int n = min(n0 + row, NN - 1);
      xv[k] = *(const u32x4*)(x + (size_t)n * 128 + ch * 8);
      wv4[k] = *(const u32x4*)(w1 + (size_t)row * 256 + half * 128 + ch * 8);
    }
#pragma unroll
    for (int k = 0; k < 4; ++k) {
      const int q = tid + 512 * k, row = q >> 4, ch = q & 15;
      lds_st<u32x4>(XO + row * 256 + ((ch ^ (row & 15)) << 4), xv[k]);
      lds_st<u32x4>(WO + row * 256 + ((ch ^ (((row & 3) << 2) | ((row >> 2) & 3))) << 4), wv4[k]);
    }
    if (tid == 0) lds_st<unsigned>(lb + 65536, 0u);
    __syncthreads();
    PSTAMP(1);
    const int nt = wv & 3, cp = wv >> 2;
    const int r = lane & 31, h = lane >> 5, bk = (lane >> 4) & 1, q4 = (lane & 15) >> 2, p4 = lane & 3;
    f32x16 acc[2];
#pragma unroll
    for (int i = 0; i < 16; ++i) { acc[0][i] = 0.f; acc[1][i] = 0.f; }
    const int xrow = 32 * nt + r;
    const unsigned xb = XO + xrow * 256 + ((h ^ (xrow & 15)) << 4);
    const int chw = 8 * cp + 2 * bk + (p4 >> 1), mA = (q4 << 2) | (2 * h), mB = mA | 1;
    const unsigned wa0 = WO + (8 * h + q4) * 256 + ((chw ^ mA) << 4) + 8 * (p4 & 1);
    const unsigned wa1 = WO + (8 * h + q4 + 4) * 256 + ((chw ^ mB) << 4) + 8 * (p4 & 1);
#pragma unroll
    for (int ks = 0; ks < 8; ++ks) {
      const u32x4 bfr = lds_ld<u32x4>(xb ^ (unsigned)(ks << 5));
#pragma unroll
      for (int ct = 0; ct < 2; ++ct) {
        const s16x4 lo = __builtin_amdgcn_ds_read_tr16_b64_v4i16((LDS_AS s16x4*)(size_t)((wa0 ^ (unsigned)(ct << 6)) + ks * 4096));
        const s16x4 hi = __builtin_amdgcn_ds_read_tr16_b64_v4i16((LDS_AS s16x4*)(size_t)((wa1 ^ (unsigned)(ct << 6)) + ks * 4096));
        const bf16x8 afr = __builtin_shufflevector(lo, hi, 0, 1, 2, 3, 4, 5, 6, 7);
        acc[ct] = mfma(__builtin_bit_cast(u32x4, afr), bfr, acc[ct]);
      }
    }
    {
      int mi = 0;
      unsigned mu = 0;
#pragma unroll
      for (int i = 0; i < 16; ++i) {
        const unsigned a = __float_as_uint(acc[0][i]), b = __float_as_uint(acc[1][i]);
        mi = max(mi, max((int)a, (int)b));
        mu = max(mu, max(a, b));
      }
      const unsigned m = wave_max_u32(max((unsigned)mi, mu & 0x7FFFFFFFu));
      if (lane == 63) lds_max(lb + 65536, m);
    }
    __syncthreads();
    PSTAMP(2);
#pragma unroll
    for (int ct = 0; ct < 2; ++ct) {
      const float sc = ct ? LOG2E : LSCALE;
#pragma unroll
      for (int g = 0; g < 4; ++g) {
        u32x2 pk;
        pk[0] = pack_bf16(acc[ct][4 * g] * sc, acc[ct][4 * g + 1] * sc);
        pk[1] = pack_bf16(acc[ct][4 * g + 2] * sc, acc[ct][4 * g + 3] * sc);
        lds_st<u32x2>(lb + xrow * 272 + (64 * cp + 32 * ct + 8 * g + 4 * h) * 2, pk);
      }
    }
    __syncthreads();
    if (tid == 0) ((unsigned*)(ws + WS_MX))[blk] = lds_ld<unsigned>(lb + 65536);
#pragma unroll
    for (int k = 0; k < 4; ++k) {
      const int q = tid + 512 * k, row = q >> 4, ch = q & 15;
      __builtin_amdgcn_raw_buffer_store_b128(lds_ld<u32x4>(lb + row * 272 + (ch << 4)), srdPw, (n0 + row) * 512 + half * 256 + ch * 16, 0, STORE_AUX);
    }
  } else if (blk < PB_T2) {
    const int bi = blk - PB_T1;
    const int b = ((blk & 7) << 5) + (bi >> 3);
    const unsigned HO = lb, SO = lb + 2048, RO = lb + 4096;
    const __amdgpu_buffer_rsrc_t srdRw = __builtin_amdgcn_make_buffer_rsrc((void*)(ws + WS_REC + (size_t)b * CHP * 12), (short)0, CHP * 12, 0x00020000);
    lds_st<unsigned>(HO + tid * 4, 0u);
    if (tid == 0) lds_st<unsigned>(SO + 128, 0u);
    int dv[4], sv[4];
    u32x2 ev[4];
#pragma unroll
    for (int k = 0; k < 4; ++k) {
      const int i = tid + 512 * k, e = b * CH + i;
      const bool ok = i < CH && e < NE;
      const int ec = ok ? e : 0;
      dv[k] = dst[ec];
      sv[k] = src[ec];
      ev[k] = *(const u32x2*)(ea + (size_t)ec * 2);
      if (!ok) dv[k] = -1;
    }
    __syncthreads();
    PSTAMP(1);
    int gk[4];
    unsigned rk[4];
#pragma unroll
    for (int k = 0; k < 4; ++k) {
      gk[k] = dv[k] >= 0 ? (int)(((unsigned)dv[k] * NG) / NN) : NG;
      rk[k] = __hip_atomic_fetch_add((LDS_AS unsigned*)(size_t)(dv[k] >= 0 ? HO + gk[k] * 4 : SO + 64), 1u, __ATOMIC_RELAXED, __HIP_MEMORY_SCOPE_WORKGROUP);
    }
    {
      u16x2 pm = {0, 0};
#pragma unroll
      for (int k = 0; k < 4; ++k) {
        pm = __builtin_elementwise_max(pm, __builtin_bit_cast(u16x2, ev[k][0] & 0x7FFF7FFFu));
        pm = __builtin_elementwise_max(pm, __builtin_bit_cast(u16x2, ev[k][1] & 0x7FFF7FFFu));
      }
      const unsigned m = wave_max_u32((unsigned)max(pm[0], pm[1]) << 16);
      if (lane == 63) lds_max(SO + 128, m);
    }
    __syncthreads();
    PSTAMP(2);
    const unsigned c = lds_ld<unsigned>(HO + tid * 4);
    const unsigned inc = wave_incl_scan(c);
    if (lane == 63) lds_st<unsigned>(SO + wv * 4, inc);
    __syncthreads();
    const u32x4 t0 = lds_ld<u32x4>(SO), t1 = lds_ld<u32x4>(SO + 16);
    unsigned pre = 0;
    pre += wv > 0 ? t0[0] : 0u; pre += wv > 1 ? t0[1] : 0u; pre += wv > 2 ? t0[2] : 0u; pre += wv > 3 ? t0[3] : 0u;
    pre += wv > 4 ? t1[0] : 0u; pre += wv > 5 ? t1[1] : 0u; pre += wv > 6 ? t1[2] : 0u;
    const unsigned start = pre + inc - c;
    ((uint32_t*)(ws + WS_TAB))[tid * NCH + b] = start | (c << 16);
    if (tid == 0) ((unsigned*)(ws + WS_MX))[256 + b] = lds_ld<unsigned>(SO + 128);
    lds_st<unsigned>(HO + tid * 4, start);
    __syncthreads();
#pragma unroll
    for (int k = 0; k < 4; ++k) {
      if (dv[k] >= 0) {
        const unsigned st = lds_ld<unsigned>(HO + gk[k] * 4);
        const unsigned ra = RO + (st + rk[k]) * 12;
        lds_st<unsigned>(ra, ((unsigned)sv[k] << 9) | ((unsigned)(dv[k] - gstart(gk[k])) << 4));
        lds_st<unsigned>(ra + 4, ev[k][0]);
        lds_st<unsigned>(ra + 8, ev[k][1]);
      }
    }
    __syncthreads();
#pragma unroll
    for (int k = 0; k < 3; ++k) {
      const int i = tid + 512 * k;
      if (i < CHP * 12 / 16) __builtin_amdgcn_raw_buffer_store_b128(lds_ld<u32x4>(RO + i * 16), srdRw, i * 16, 0, STORE_AUX);
    }
  } else if (blk < PB_WB) {
    const int q = (blk - PB_T2) * 512 + tid;
    const int l = q & 63, ks = (q >> 6) & 7, ct = q >> 9;
    const int col = 32 * ct + (l & 31), k0 = 128 + 16 * ks + 8 * (l >> 5);
    unsigned short v[8];
#pragma unroll
    for (int j = 0; j < 8; ++j) v[j] = w1[(size_t)(k0 + j) * 256 + col];
    u32x4 o;
#pragma unroll
    for (int j = 0; j < 4; ++j) o[j] = v[2 * j] | ((unsigned)v[2 * j + 1] << 16);
    *(u32x4*)(ws + WS_WB + (size_t)q * 16) = o;
  } else {
    const int q = (blk - PB_WB) * 512 + tid;
    const int l = q & 63, ot = (q >> 6) & 3, ks = q >> 8;
    const int col = 32 * ot + (l & 31), h = l >> 5;
    unsigned short v[8];
#pragma unroll
    for (int j = 0; j < 8; ++j) v[j] = w2[(size_t)(32 * (ks >> 1) + crow(8 * (ks & 1) + j, h)) * 128 + col];
    u32x4 o;
#pragma unroll
    for (int j = 0; j < 4; ++j) o[j] = v[2 * j] | ((unsigned)v[2 * j + 1] << 16);
    *(u32x4*)(ws + WS_W2 + (size_t)q * 16) = o;
  }
  PSTAMP(3);
}

constexpr unsigned L_TP = 0;
constexpr unsigned L_WORD = 32768;
constexpr unsigned WORD_TEAM = 36 * 128;
constexpr unsigned L_MASK = L_WORD + 2 * WORD_TEAM;
constexpr unsigned MASK_TEAM = 34 * 128;
constexpr unsigned L_CNT = L_MASK + 2 * MASK_TEAM;
constexpr unsigned L_TOT = L_CNT + 256;
constexpr unsigned L_MAX = L_TOT + 16;
constexpr unsigned L_PROG = L_TOT + 32;
constexpr unsigned L_BKT = L_TOT + 64;
constexpr unsigned L_ZEND = L_TOT + 128;
constexpr unsigned L_ALUT = (L_ZEND + 511) / 512 * 512;
constexpr unsigned L_BLUT = L_ALUT + 2048;
constexpr unsigned L_EA = L_BLUT + 4096;
constexpr unsigned L_AEND = L_EA + 4096;
constexpr unsigned L_TN = 65536;
constexpr unsigned L_STG = 98304;
constexpr unsigned L_END = 163840;
static_assert(L_AEND <= 65536 && (L_ZEND - L_MASK) % 16 == 0 && L_ALUT % 512 == 0, "LDS region A");
constexpr unsigned PADWORD = 0x7FFFFE00u | (31u << 4);

__global__ __launch_bounds__(512, 2) void main_kernel(const unsigned short* __restrict__ x, const unsigned short* __restrict__ w1,
                                                      const unsigned short* __restrict__ b1, const unsigned short* __restrict__ b2,
                                                      char* __restrict__ ws, unsigned short* __restrict__ out) {
  __shared__ __attribute__((aligned(16))) char lds[L_END];
  asm volatile("" ::"s"((unsigned)(size_t)lds));
  const int tid = threadIdx.x, lane = tid & 63;
  const int wv = __builtin_amdgcn_readfirstlane(tid >> 6);
  const int team = wv >> 2, w = wv & 3, tt = tid & 255;
  const int wg = (blockIdx.x & 7) * 32 + (blockIdx.x >> 3);
  const int g = 2 * wg + team;
  const int n0 = gstart(g), G = gstart(g + 1) - n0;
  const int r = lane & 31, h = lane >> 5;

  const __amdgpu_buffer_rsrc_t srdP = __builtin_amdgcn_make_buffer_rsrc((void*)(ws + WS_P - 3072), (short)0, NN * 512 + 3072, 0x00020000);
  STAMP(0);
  const uint32_t tabv = ((const uint32_t*)(ws + WS_TAB))[g * NCH + tt];
  u32x4 xs[2];
#pragma unroll
  for (int k = 0; k < 2; ++k) {
    const int q = tt + 256 * k, row = q >> 4, ch = q & 15;
    const int n = min(n0 + row, NN - 1);
    xs[k] = *(const u32x4*)(x + (size_t)n * 128 + ch * 8);
  }
  unsigned mxv = ((const unsigned*)(ws + WS_MX))[tid];
  const unsigned short cb0 = b1[32 * wv + r];
  const unsigned short cb1 = w1[(size_t)(256 + 2 * h) * 256 + 32 * wv + r];
  const unsigned short cb2 = w1[(size_t)(257 + 2 * h) * 256 + 32 * wv + r];

  STAMP(1);
  {
    const u32x4 pad = {PADWORD, PADWORD, PADWORD, PADWORD}, zero = {0, 0, 0, 0};
    lds_st<u32x4>(L_WORD + tid * 16, pad);
    if (tid < (int)(2 * WORD_TEAM / 16 - 512)) lds_st<u32x4>(L_WORD + (512 + tid) * 16, pad);
    lds_st<u32x4>(L_MASK + tid * 16, zero);
    if (tid < (int)((L_ZEND - L_MASK) / 16 - 512)) lds_st<u32x4>(L_MASK + (512 + tid) * 16, zero);
  }
  if (tid < 128) {
    const int u = tid & 31, hh = (tid >> 5) & 1, s = tid >> 6;
    const int rho = u < 26 ? u : u + 2;
    const int i = (rho & 3) + 4 * (rho >> 3), j = i & 7;
    const bool on = u < 28 && ((rho >> 2) & 1) == hh && (i >> 3) == s;
    const unsigned one = on ? (0x3F80u << (16 * (j & 1))) : 0u;
    u32x4 v;
#pragma unroll
    for (int q = 0; q < 4; ++q) v[q] = (j >> 1) == q ? one : 0u;
    lds_st<u32x4>(L_ALUT + tid * 16, v);
  } else if (tid < 384) {
    const int bb = tid - 128;
    u32x4 v;
#pragma unroll
    for (int j = 0; j < 4; ++j) v[j] = ((bb >> (2 * j)) & 1 ? 0x3F80u : 0u) | ((bb >> (2 * j + 1)) & 1 ? 0x3F800000u : 0u);
    lds_st<u32x4>(L_BLUT + bb * 16, v);
  }
  STAMP(2);
  u32x4 wb[8];
#pragma unroll
  for (int k = 0; k < 8; ++k) wb[k] = *(const u32x4*)(ws + WS_WB + (size_t)(wv * 8 + k) * 1024 + lane * 16);

  const unsigned wbase = L_WORD + team * WORD_TEAM;
  const unsigned stg = L_STG + wv * 8192;
  const unsigned lc0 = (unsigned)(w * 128 + (((lane & 7) ^ ((lane >> 4) & 3)) << 4));
  const unsigned lc1 = lc0 ^ 64u;
  const unsigned w4a = wbase + (lane >> 3) * 16;
  const unsigned eav = (unsigned)((2 * r + h) * 4);
  STAMP(3);

  auto xaddr = [&](int tm, int row, int ch) { return L_STG + (unsigned)(2 * tm + (row >> 4)) * 8192 + (unsigned)(row & 15) * 256 + (unsigned)((ch ^ (row & 15)) << 4); };
  auto qaddr = [&](int tm, int ct, int s) { const int i = (tm * 8 + ct) * 2 + s; return L_STG + (unsigned)(i >> 2) * 8192 + 4096 + (unsigned)(i & 3) * 1024 + lane * 16; };
  const unsigned IDX = L_STG + (4 + team) * 8192;
#pragma unroll
  for (int k = 0; k < 2; ++k) {
    const int q = tt + 256 * k, row = q >> 4, ch = q & 15;
    u32x4 v = xs[k];
    if (row >= G) v = u32x4{0, 0, 0, 0};
    lds_st<u32x4>(xaddr(team, row, ch), v);
  }
  STAMP(4);
  __syncthreads();

  STAMP(5);
  const unsigned segs = tabv & 0xFFFFu, segc = tabv >> 16;
  const unsigned inc = wave_incl_scan(segc);
  unsigned wbase0 = 0;
  if (lane == 63) wbase0 = __hip_atomic_fetch_add((LDS_AS unsigned*)(size_t)(L_TOT + team * 4), inc, __ATOMIC_RELAXED, __HIP_MEMORY_SCOPE_WORKGROUP);
  const unsigned pos0 = __builtin_amdgcn_readlane(wbase0, 63) + inc - segc;
  const unsigned rec0 = (unsigned)tt * CHP + segs;
  char* eap = ws + WS_EA + (size_t)g * CAP * 8;
  {
    const unsigned kend = pos0 < (unsigned)CAP ? min(segc, (unsigned)CAP - pos0) : 0u;
    for (unsigned k = 0; k < kend; ++k) lds_st<unsigned>(IDX + (pos0 + k) * 4, rec0 + k);
  }
  STAMP(6);
  __syncthreads();

  unsigned total;
  int nrounds;
  {
    const unsigned t0 = lds_ld<unsigned>(L_TOT), t1 = lds_ld<unsigned>(L_TOT + 4);
    total = __builtin_amdgcn_readfirstlane(team ? t1 : t0);
    nrounds = __builtin_amdgcn_readfirstlane((int)((max(t0, t1) + CAP - 1) / CAP));
  }
  STAMP(7);
  u32x3 rec[4];
  auto fetch_records = [&](unsigned rem) {
#pragma unroll
    for (int k = 0; k < 4; ++k) {
      const unsigned p = tt + 256 * k;
      const unsigned ri = lds_ld<unsigned>(IDX + p * 4);
      rec[k] = *(const u32x3_a4*)(ws + WS_REC + (size_t)(p < rem ? ri : 0u) * 12);
    }
  };
  fetch_records(min(total, (unsigned)CAP));
  const unsigned pfv = *(const unsigned*)(ws + WS_P + (size_t)((blockIdx.x >> 3) * PREF_LINES + min(tid, PREF_LINES - 1)) * 128);
  STAMP(8);
  {
    const int u = r < 26 ? r : (r == 28 || r == 29) ? r - 2 : 31;
    const float sc = (wv & 1) ? LOG2E : LSCALE;
    const float cin0 = bf2f(cb0), cin1 = bf2f(cb1), cin2 = bf2f(cb2);
    f32x16 acc[2];
    u32x4 af[2][8];
#pragma unroll
    for (int tm = 0; tm < 2; ++tm) {
#pragma unroll
      for (int ks = 0; ks < 8; ++ks) af[tm][ks] = lds_ld<u32x4>(xaddr(tm, u, 2 * ks + h));
#pragma unroll
      for (int i = 0; i < 14; ++i) acc[tm][i] = cin0;
      acc[tm][14] = cin1;
      acc[tm][15] = cin2;
    }
    __builtin_amdgcn_sched_barrier(0);
#pragma unroll
    for (int ks = 0; ks < 8; ++ks) {
      acc[0] = mfma(af[0][ks], wb[ks], acc[0]);
      acc[1] = mfma(af[1][ks], wb[ks], acc[1]);
    }
    int mi = 0;
    unsigned mu = 0;
#pragma unroll
    for (int tm = 0; tm < 2; ++tm) {
#pragma unroll
      for (int i = 0; i < 14; i += 2) {
        const unsigned a = __float_as_uint(acc[tm][i]), b = __float_as_uint(acc[tm][i + 1]);
        mi = max(mi, max((int)a, (int)b));
        mu = max(mu, max(a, b));
      }
#pragma unroll
      for (int s = 0; s < 2; ++s) {
        u32x4 v;
#pragma unroll
        for (int j = 0; j < 4; ++j) v[j] = pack_bf16(acc[tm][8 * s + 2 * j] * sc, acc[tm][8 * s + 2 * j + 1] * sc);
        lds_st<u32x4>(qaddr(tm, wv, s), v);
      }
    }
    const unsigned mq = wave_max_u32(max((unsigned)mi, mu & 0x7FFFFFFFu));
    const unsigned mw = wave_max_u32(max(absbits(cin1), absbits(cin2)));
    const unsigned mx = wave_max_u32(tid < PB_T1 || tid >= 256 ? mxv : 0u);
    if (lane == 63) { lds_max(L_MAX + (team ? 4 : 0), mx); lds_max(L_MAX + 8, mq); lds_max(L_MAX + 12, mw); }
  }
  STAMP(9);
  f32x16 S[2];
#pragma unroll
  for (int i = 0; i < 16; ++i) { S[0][i] = 0.f; S[1][i] = 0.f; }
  auto place_at = [&](unsigned p, const u32x3& rc) {
    const unsigned tile = p >> 5, rr = p & 31;
    lds_st<unsigned>(wbase + tile * 128 + ((rr & 7) * 4 + (rr >> 3)) * 4, rc[0]);
    const unsigned u = (rc[0] >> 4) & 31;
    const unsigned bit = 16 * (rr >> 4) + 8 * ((rr >> 2) & 1) + 4 * ((rr >> 3) & 1) + (rr & 3);
    __hip_atomic_fetch_or((LDS_AS unsigned*)(size_t)(L_MASK + team * MASK_TEAM + tile * 128 + u * 4), 1u << bit, __ATOMIC_RELAXED, __HIP_MEMORY_SCOPE_WORKGROUP);
    __hip_atomic_fetch_add((LDS_AS unsigned*)(size_t)(L_CNT + team * 128 + u * 4), 1u, __ATOMIC_RELAXED, __HIP_MEMORY_SCOPE_WORKGROUP);
    *(u32x2*)(eap + (size_t)p * 8) = u32x2{rc[1], rc[2]};
  };
  const unsigned rem0 = min(total, (unsigned)CAP);
  {
    unsigned bk[4], rk[4];
#pragma unroll
    for (int k = 0; k < 4; ++k) {
      bk[k] = ((rec[k][0] >> 9) * 8u) / (unsigned)NN;
      rk[k] = 0;
      if (tt + 256u * k < rem0) rk[k] = __hip_atomic_fetch_add((LDS_AS unsigned*)(size_t)(L_BKT + team * 32 + bk[k] * 4), 1u, __ATOMIC_RELAXED, __HIP_MEMORY_SCOPE_WORKGROUP);
    }
    asm volatile("s_waitcnt lgkmcnt(0)" ::: "memory");
    __builtin_amdgcn_s_barrier();
    const unsigned cnt8 = lds_ld<unsigned>(L_BKT + team * 32 + (lane & 7) * 4);
    const unsigned pre8 = wave_incl_scan(lane < 8 ? cnt8 : 0u) - cnt8;
#pragma unroll
    for (int k = 0; k < 4; ++k) {
      const unsigned base = __builtin_amdgcn_ds_bpermute(bk[k] * 4, pre8);
      if (tt + 256u * k < rem0) place_at(base + rk[k], rec[k]);
    }
  }
  STAMP(10);
  asm volatile("s_waitcnt lgkmcnt(0)" ::: "memory");
  __builtin_amdgcn_s_barrier();
  STAMP(11);

  auto dmaP = [&](int B, u32x4 wd) {
    const unsigned v0 = (wd[0] & 0xFFFFFE00u) | lc0, v1 = (wd[1] & 0xFFFFFE00u) | lc1;
    const unsigned v2 = (wd[2] & 0xFFFFFE00u) | lc0, v3 = (wd[3] & 0xFFFFFE00u) | lc1;
    const unsigned m0p = stg + B * 4096;
    asm volatile(
        "s_mov_b32 m0, %4\n\ts_nop 0\n\tbuffer_load_dwordx4 %0, %5, %6 offen lds\n\t"
        "buffer_load_dwordx4 %1, %5, %7 offen offset:1024 lds\n\t"
        "buffer_load_dwordx4 %2, %5, %8 offen offset:2048 lds\n\t"
        "buffer_load_dwordx4 %3, %5, 0 offen offset:3072 lds"
        ::"v"(v0), "v"(v1), "v"(v2), "v"(v3), "s"(m0p), "s"(srdP), "s"(3072), "s"(2048), "s"(1024)
        : "memory");
  };
  auto dmaE = [&](int T, int B, __amdgpu_buffer_rsrc_t srdE) {
    const unsigned ve = eav + (unsigned)T * 256u;
    const unsigned m0e = L_EA + wv * 512 + B * 256;
    asm volatile("s_mov_b32 m0, %1\n\ts_nop 0\n\tbuffer_load_dword %0, %2, 0 offen lds" ::"v"(ve), "s"(m0e), "s"(srdE) : "memory");
  };
  const __amdgpu_buffer_rsrc_t srdE0 = __builtin_amdgcn_make_buffer_rsrc((void*)eap, (short)0, (int)(rem0 * 8), 0x00020000);
  dmaP(0, lds_ld<u32x4>(w4a));

  u32x4 qf[2][2];
#pragma unroll
  for (int t = 0; t < 2; ++t)
#pragma unroll
    for (int s = 0; s < 2; ++s) qf[t][s] = lds_ld<u32x4>(qaddr(team, 2 * w + t, s));
  bool fast;
  {
    const u32x4 mx = lds_ld<u32x4>(L_MAX);
    const float bound = __uint_as_float(mx[0]) + __uint_as_float(mx[2]) + 4.0f * __uint_as_float(mx[1]) * __uint_as_float(mx[3]);
    fast = __builtin_amdgcn_readfirstlane(bound < 120.0f ? 1 : 0) != 0 && nrounds <= 1;
  }
  asm volatile("" ::"v"(qf[0][0]), "v"(qf[0][1]), "v"(qf[1][0]), "v"(qf[1][1]));
  STAMP(13);
  constexpr unsigned TINY = 110u * 128u;
#pragma unroll
  for (int k = 0; k < 4; ++k) {
    const unsigned c0 = (unsigned)(k * 512 + tid) * 8;
    if (k < 3 || c0 < TINY) {
      u32x4 tp;
#pragma unroll
      for (int j = 0; j < 4; ++j) {
        const unsigned b0 = ((c0 + 2 * j) << 16) | 0x8000u;
        const f32x2_t v = f32x2_t{__uint_as_float(b0), __uint_as_float(b0 + 0x10000u)} * 32.0f;
        tp[j] = pack_bf16(v[0], v[1]);
      }
      lds_st<u32x4>(L_TP + c0 * 2, tp);
      lds_st<u32x4>(L_TN + c0 * 2, tp | 0x80008000u);
    }
  }
  for (unsigned c0 = TINY + tid * 4; c0 < 16384u; c0 += 2048u) {
    u32x2 tp, tn;
#pragma unroll
    for (int j = 0; j < 2; ++j) {
      const unsigned b0 = ((c0 + 2 * j) << 16) | 0x8000u;
      const float za = __uint_as_float(b0) * 64.0f, zb = __uint_as_float(b0 + 0x10000u) * 64.0f;
      const float ea_ = __builtin_amdgcn_exp2f(-LOG2E * za), eb_ = __builtin_amdgcn_exp2f(-LOG2E * zb);
      const float ra = __builtin_amdgcn_rcpf(1.0f + ea_), rb = __builtin_amdgcn_rcpf(1.0f + eb_);
      tp[j] = pack_bf16(za * ra, zb * rb);
      tn[j] = pack_bf16(-(za * ea_) * ra, -(zb * eb_) * rb);
    }
    lds_st<u32x2>(L_TP + c0 * 2, tp);
    lds_st<u32x2>(L_TN + c0 * 2, tn);
  }
  STAMP(19);
  u32x4 idn[2];
#pragma unroll
  for (int s = 0; s < 2; ++s) {
    const unsigned d = (unsigned)(r - 16 * s - 8 * h);
    idn[s] = lds_ld<u32x4>(L_BLUT + (d < 8u ? (16u << d) : 0u));
  }
  const unsigned pb = (unsigned)(r * 128 + ((h ^ ((r >> 1) & 7)) << 4));
  unsigned pa0 = stg + pb, pa1 = stg + (pb ^ 32u), pa2 = stg + (pb ^ 64u), pa3 = stg + (pb ^ 96u);
  unsigned w1a = wbase + ((r & 7) * 4 + (r >> 3)) * 4;
  unsigned mka = L_MASK + team * MASK_TEAM + r * 4 + h;
  unsigned eal = L_EA + wv * 512 + lane * 4;
  unsigned alh = L_ALUT + h * 512;
  asm volatile("" : "+v"(idn[0]), "+v"(idn[1]), "+v"(pa0), "+v"(pa1), "+v"(pa2), "+v"(pa3), "+v"(w1a), "+v"(mka), "+v"(eal), "+v"(alh));
  {
    const u32x4 wd_1 = lds_ld<u32x4>(w4a + 128);
    STAMP(20);
    asm volatile("" ::"v"(pfv));
    asm volatile("s_waitcnt vmcnt(4) lgkmcnt(0)" ::: "memory");
    STAMP(21);
    __builtin_amdgcn_s_barrier();
    dmaE(0, 0, srdE0);
    dmaP(1, wd_1);
    dmaE(1, 1, srdE0);
  }
  STAMP(12);
  auto load_ops = [&](unsigned wd, int B, u32x4 (&pp)[4], u32x4 (&aa)[2]) {
    const unsigned ad = (wd & 0x1F0u) | alh;
    aa[0] = lds_ld<u32x4>(ad);
    const u32x3 a1 = lds_ld<u32x3>(ad + 1024);
    aa[1][0] = a1[0]; aa[1][1] = a1[1]; aa[1][2] = a1[2];
    aa[1][3] = lds_ld<unsigned>(eal + B * 256);
    pp[0] = lds_ld<u32x4>(pa0 + B * 4096);
    pp[1] = lds_ld<u32x4>(pa1 + B * 4096);
    pp[2] = lds_ld<u32x4>(pa2 + B * 4096);
    pp[3] = lds_ld<u32x4>(pa3 + B * 4096);
  };
#define SB() __builtin_amdgcn_sched_barrier(0)
#define KEEP_OPS() asm volatile("" ::"v"(pp[0]), "v"(pp[1]), "v"(pp[2]), "v"(pp[3]), "v"(aa[1]))
  f32x16 zz;
#pragma unroll
  for (int i = 0; i < 16; ++i) zz[i] = 0.f;

  if (fast) {
    const int ntiles = (int)((rem0 + 31) >> 5);
    if (ntiles > 0) {
      u32x4 pp[4], aa[2];
      asm volatile("s_waitcnt vmcnt(5)" ::: "memory");
      STAMP(14);
      load_ops(lds_ld<unsigned>(w1a), 0, pp, aa);
      {
        const u32x4 wd = lds_ld<u32x4>(w4a + 256);
        KEEP_OPS();
        dmaP(0, wd);
        dmaE(2, 0, srdE0);
      }
      STAMP(15);
      CSTAMP(30);
      u32x4 bp[2], bn[2], h0[2], h1[2], wd4;
      f32x16 z0, z1, z1p;
      unsigned lk[16], mb0, mb1, wd1, orem;
      f32x2_t E[8], R[8], M[8];
#pragma unroll
      for (int i = 0; i < 16; ++i) { z1p[i] = 0.f; lk[i] = 0; }
      bp[0] = u32x4{0, 0, 0, 0};
      bp[1] = bp[0];
#define VE(j) E[j] = f32x2_t{__builtin_amdgcn_exp2f(-z1p[2 * (j)]), __builtin_amdgcn_exp2f(-z1p[2 * (j) + 1])}
#define VA(j) E[j] = E[j] + 1.0f
#define VR(j) R[j] = f32x2_t{__builtin_amdgcn_rcpf(E[j][0]), __builtin_amdgcn_rcpf(E[j][1])}
#define VM(j) M[j] = f32x2_t{z1p[2 * (j)], z1p[2 * (j) + 1]} * R[j]
#define VC(j) h1[(j) >> 2][(j) & 3] = pack_bf16(M[j][0], M[j][1])
#define MG(j) h0[(j) >> 2][(j) & 3] = lk[2 * (j)] | (lk[2 * (j) + 1] << 16)
#define LL(i) lk[i] = lds_ld<unsigned short>((__float_as_uint(z0[i]) >> 16) << 1)
      auto partA = [&](int T) {
        z0 = mfma(aa[0], qf[0][0], zz); VE(0); MG(0); SB();
        z0 = mfma(aa[1], qf[0][1], z0); VE(1); VA(0); MG(1); SB();
        z0 = mfma(pp[0], idn[0], z0); VE(2); VA(1); VR(0); MG(2);
        mb0 = lds_ld<unsigned char>(mka + T * 128); mb1 = lds_ld<unsigned char>(mka + T * 128 + 2);
        wd1 = lds_ld<unsigned>(w1a + (T + 1) * 128); wd4 = lds_ld<u32x4>(w4a + (T + 3) * 128);
        lds_st<unsigned>(L_PROG + wv * 4, (unsigned)(ntiles - T)); orem = lds_ld<unsigned>(L_PROG + (wv ^ 4) * 4);
        SB();
        z0 = mfma(pp[1], idn[1], z0); VE(3); VA(2); VR(1); VM(0); MG(3); SB();
        z1 = mfma(aa[0], qf[1][0], zz); VE(4); VA(3); VR(2); VM(1); VC(0); MG(4); SB();
      };
      auto partB = [&](int T, auto BC) {
        constexpr int B = decltype(BC)::value;
        u32x4 pn[4], an[2];
        asm volatile("s_waitcnt vmcnt(5)" ::: "memory");
        load_ops(wd1, B, pn, an);
        z1 = mfma(aa[1], qf[1][1], z1); VE(5); VA(4); VR(3); VM(2); VC(1); MG(5); LL(0); LL(1); SB();
        z1 = mfma(pp[2], idn[0], z1); VE(6); VA(5); VR(4); VM(3); VC(2); MG(6); LL(2); LL(3); SB();
        z1 = mfma(pp[3], idn[1], z1); VE(7); VA(6); VR(5); VM(4); VC(3); MG(7); LL(4); LL(5);
        asm volatile("" ::"v"(pn[0]), "v"(pn[1]), "v"(pn[2]), "v"(pn[3]), "v"(an[1]));
        dmaP(B, wd4);
        dmaE(T + 3, B, srdE0);
        SB();
        S[0] = mfma(h0[0], bp[0], S[0]); VA(7); VR(6); VM(5); VC(4); LL(6); LL(7); LL(8); SB();
        S[0] = mfma(h0[1], bp[1], S[0]); VR(7); VM(6); VC(5); LL(9); LL(10); LL(11);
        bn[0] = lds_ld<u32x4>(L_BLUT + mb0 * 16); bn[1] = lds_ld<u32x4>(L_BLUT + mb1 * 16);
        SB();
        S[1] = mfma(h1[0], bp[0], S[1]); VM(7); VC(6); LL(12); LL(13); LL(14); SB();
        VC(7); LL(15);
        S[1] = mfma(h1[1], bp[1], S[1]);
        {
          const int mine = ntiles - T, other = __builtin_amdgcn_readfirstlane((int)orem);
          asm volatile("s_cmp_gt_i32 %0, %1\n\ts_cbranch_scc1 1f\n\ts_setprio 0\n\ts_branch 2f\n1:\n\ts_setprio 1\n2:" ::"s"(mine), "s"(other) : "scc");
        }
        SB();
        z1p = z1;
        bp[0] = bn[0];
        bp[1] = bn[1];
        pp[0] = pn[0]; pp[1] = pn[1]; pp[2] = pn[2]; pp[3] = pn[3];
        aa[0] = an[0]; aa[1] = an[1];
      };
      const std::integral_constant<int, 0> B0;
      const std::integral_constant<int, 1> B1;
      partA(0);
      int t = 0;
      for (; t + 2 < ntiles; t += 2) {
        partB(t, B1);
        partA(t + 1);
        partB(t + 1, B0);
        partA(t + 2);
      }
      partB(t, B1);
      if (t + 1 < ntiles) {
        partA(t + 1);
        partB(t + 1, B0);
      }
#pragma unroll
      for (int j = 0; j < 8; ++j) { VE(j); VA(j); VR(j); VM(j); VC(j); MG(j); }
      S[0] = mfma(h0[0], bp[0], S[0]);
      S[1] = mfma(h1[0], bp[0], S[1]);
      S[0] = mfma(h0[1], bp[1], S[0]);
      S[1] = mfma(h1[1], bp[1], S[1]);
      __builtin_amdgcn_s_setprio(0);
      if (lane == 0) lds_st<unsigned>(L_PROG + wv * 4, 0u);
    }
    asm volatile("s_waitcnt vmcnt(0)" ::: "memory");
  } else {
    int round = 0;
    do {
      const unsigned lo = (unsigned)round * CAP;
      const unsigned rem = total > lo ? min(total - lo, (unsigned)CAP) : 0u;
      const __amdgpu_buffer_rsrc_t srdE = __builtin_amdgcn_make_buffer_rsrc((void*)eap, (short)0, (int)(rem * 8), 0x00020000);
      if (round > 0) {
        __syncthreads();
        for (int i = tt; i < 36 * 32; i += 256) lds_st<unsigned>(wbase + i * 4, PADWORD);
        for (int i = tt; i < 34 * 32; i += 256) lds_st<unsigned>(L_MASK + team * MASK_TEAM + i * 4, 0u);
        for (unsigned k = 0; k < segc; ++k) {
          const unsigned p = pos0 + k - lo;
          if (p < (unsigned)CAP) lds_st<unsigned>(IDX + p * 4, rec0 + k);
        }
        __syncthreads();
        fetch_records(rem);
#pragma unroll
        for (int k = 0; k < 4; ++k)
          if (tt + 256u * k < rem) place_at(tt + 256u * k, rec[k]);
        asm volatile("s_waitcnt vmcnt(0)" ::: "memory");
        __syncthreads();
        dmaP(0, lds_ld<u32x4>(w4a));
        dmaE(0, 0, srdE);
        dmaP(1, lds_ld<u32x4>(w4a + 128));
        dmaE(1, 1, srdE);
      }
      const int ntiles = (int)((rem + 31) >> 5);
      if (ntiles > 0) {
        u32x4 pp[4], aa[2];
        asm volatile("s_waitcnt vmcnt(5)" ::: "memory");
        load_ops(lds_ld<unsigned>(w1a), 0, pp, aa);
        {
          const u32x4 wd = lds_ld<u32x4>(w4a + 256);
          KEEP_OPS();
          dmaP(0, wd);
          dmaE(2, 0, srdE);
        }
        for (int t = 0; t < ntiles; ++t) {
          const int B = (t + 1) & 1;
          f32x16 z0 = mfma(aa[0], qf[0][0], zz), z1 = mfma(aa[0], qf[1][0], zz);
          z0 = mfma(aa[1], qf[0][1], z0);
          z1 = mfma(aa[1], qf[1][1], z1);
          z0 = mfma(pp[0], idn[0], z0);
          z1 = mfma(pp[2], idn[0], z1);
          z0 = mfma(pp[1], idn[1], z0);
          z1 = mfma(pp[3], idn[1], z1);
          u32x4 bb[2], h0[2], h1[2];
          bb[0] = lds_ld<u32x4>(L_BLUT + lds_ld<unsigned char>(mka + t * 128) * 16);
          bb[1] = lds_ld<u32x4>(L_BLUT + lds_ld<unsigned char>(mka + t * 128 + 2) * 16);
#pragma unroll
          for (int j = 0; j < 8; ++j) {
            const float za = z0[2 * j] * 64.0f, zb = z0[2 * j + 1] * 64.0f, ya = z1[2 * j], yb = z1[2 * j + 1];
            h0[j >> 2][j & 3] = pack_bf16(za * __builtin_amdgcn_rcpf(1.0f + __builtin_amdgcn_exp2f(-LOG2E * za)), zb * __builtin_amdgcn_rcpf(1.0f + __builtin_amdgcn_exp2f(-LOG2E * zb)));
            h1[j >> 2][j & 3] = pack_bf16(ya * __builtin_amdgcn_rcpf(1.0f + __builtin_amdgcn_exp2f(-ya)), yb * __builtin_amdgcn_rcpf(1.0f + __builtin_amdgcn_exp2f(-yb)));
          }
          S[0] = mfma(h0[0], bb[0], S[0]);
          S[1] = mfma(h1[0], bb[0], S[1]);
          S[0] = mfma(h0[1], bb[1], S[0]);
          S[1] = mfma(h1[1], bb[1], S[1]);
          asm volatile("s_waitcnt vmcnt(5)" ::: "memory");
          const u32x4 wd = lds_ld<u32x4>(w4a + (t + 3) * 128);
          load_ops(lds_ld<unsigned>(w1a + (t + 1) * 128), B, pp, aa);
          KEEP_OPS();
          dmaP(B, wd);
          dmaE(t + 3, B, srdE);
        }
      }
      asm volatile("s_waitcnt vmcnt(0)" ::: "memory");
    } while (++round < nrounds);
  }

  STAMP(16);
  CSTAMP(31);
  u32x4 w2f[16];
#pragma unroll
  for (int ks = 0; ks < 16; ++ks) w2f[ks] = *(const u32x4*)(ws + WS_W2 + (size_t)(ks * 4 + w) * 1024 + lane * 16);
  u32x2 b2v[4];
#pragma unroll
  for (int gq = 0; gq < 4; ++gq) b2v[gq] = *(const u32x2*)(b2 + 32 * w + 8 * gq + 4 * h);
  __syncthreads();
  const unsigned EX = L_STG + team * 32768;
#pragma unroll
  for (int t = 0; t < 2; ++t) {
    const float sc = t ? LN2 : 1.0f;
#pragma unroll
    for (int s = 0; s < 2; ++s) {
      u32x4 v;
#pragma unroll
      for (int j = 0; j < 4; ++j) v[j] = pack_bf16(S[t][8 * s + 2 * j] * sc, S[t][8 * s + 2 * j + 1] * sc);
      lds_st<u32x4>(EX + (4 * w + 2 * t + s) * 1024 + lane * 16, v);
    }
  }
  __syncthreads();
  STAMP(17);
  const float deg = (float)lds_ld<unsigned>(L_CNT + team * 128 + r * 4);
  f32x16 o;
#pragma unroll
  for (int gq = 0; gq < 4; ++gq) {
    o[4 * gq + 0] = deg * __uint_as_float(b2v[gq][0] << 16);
    o[4 * gq + 1] = deg * __uint_as_float(b2v[gq][0] & 0xFFFF0000u);
    o[4 * gq + 2] = deg * __uint_as_float(b2v[gq][1] << 16);
    o[4 * gq + 3] = deg * __uint_as_float(b2v[gq][1] & 0xFFFF0000u);
  }
#pragma unroll
  for (int ks = 0; ks < 16; ++ks) o = mfma(w2f[ks], lds_ld<u32x4>(EX + ks * 1024 + lane * 16), o);
  if (r < G) {
    unsigned short* op = out + (size_t)(n0 + r) * 128 + 32 * w + 4 * h;
#pragma unroll
    for (int gq = 0; gq < 4; ++gq) {
      u32x2 v;
      v[0] = pack_bf16(o[4 * gq], o[4 * gq + 1]);
      v[1] = pack_bf16(o[4 * gq + 2], o[4 * gq + 3]);
      *(u32x2*)(op + 8 * gq) = v;
    }
  }
  STAMP(18);
}

}

extern "C" void kernel_launch(void* const* d_in, const int* in_sizes, int n_in, void* d_out, int out_size, void* d_ws,
                              size_t ws_size, hipStream_t stream) {
  const unsigned short* x = (const unsigned short*)d_in[0];
  const uint32_t* ea = (const uint32_t*)d_in[1];
  const int* src = (const int*)d_in[2];
  const int* dst = (const int*)d_in[3];
  const unsigned short* w1 = (const unsigned short*)d_in[4];
  const unsigned short* b1 = (const unsigned short*)d_in[5];
  const unsigned short* w2 = (const unsigned short*)d_in[6];
  const unsigned short* b2 = (const unsigned short*)d_in[7];
  (void)in_sizes; (void)n_in; (void)out_size; (void)ws_size;
  prep_kernel<<<PB_W2, 512, 0, stream>>>(x, ea, src, dst, w1, w2, (char*)d_ws);
  main_kernel<<<MAIN_GRID, 512, 0, stream>>>(x, w1, b1, b2, (char*)d_ws, (unsigned short*)d_out);
}
```

```cpp
#include <hip/hip_runtime.h>
#include <hip/hip_bf16.h>
#include <stdint.h>
#include <type_traits>

#define ABL 0
#define MAIN_GRID 256
#define STORE_AUX 16
#define PREF 0
#define PREF_LINES 196
#define PRIO 0
namespace {

constexpr int NN = 12500;
constexpr int NE = 400000;
constexpr int NG = 512;
constexpr int NCH = 256;
constexpr int CH = 1563;
constexpr int CHP = 1568;
constexpr int CAP = 1024;

constexpr size_t WS_P = 4096;
constexpr size_t WS_REC = WS_P + 6400000;
constexpr size_t WS_TAB = WS_REC + (size_t)NCH * CHP * 12;
constexpr size_t WS_WB = WS_TAB + (size_t)NCH * NG * 4;
constexpr size_t WS_W2 = WS_WB + 65536;
constexpr size_t WS_MX = WS_W2 + 65536;
constexpr size_t WS_EA = WS_MX + 2048;
constexpr size_t WS_END = WS_EA + (size_t)NG * CAP * 8;
#define STAMP(k) do { } while (0)
#define CSTAMP(k) do { } while (0)
#define PSTAMP(k) do { } while (0)

constexpr float LOG2E = 1.4426950408889634f;
constexpr float LN2 = 0.6931471805599453f;
constexpr float LSCALE = 1.0f / 64.0f;

typedef __attribute__((ext_vector_type(8))) short bf16x8;
typedef __attribute__((ext_vector_type(4))) short s16x4;
typedef __attribute__((ext_vector_type(16))) float f32x16;
typedef __attribute__((ext_vector_type(4))) unsigned u32x4;
typedef __attribute__((ext_vector_type(2))) unsigned u32x2;
typedef __attribute__((ext_vector_type(3))) unsigned u32x3;
typedef __attribute__((ext_vector_type(3), aligned(4))) unsigned u32x3_a4;
typedef __attribute__((ext_vector_type(2))) unsigned short u16x2;
typedef __bf16 bf16x2_t __attribute__((ext_vector_type(2)));
typedef float f32x2_t __attribute__((ext_vector_type(2)));
#define LDS_AS __attribute__((address_space(3)))
#define DI __device__ __forceinline__

DI unsigned pack_bf16(float a, float b) {
  f32x2_t v = {a, b};
  bf16x2_t r = __builtin_convertvector(v, bf16x2_t);
  return __builtin_bit_cast(unsigned, r);
}
DI float bf2f(unsigned short u) { return __uint_as_float((unsigned)u << 16); }
DI f32x16 mfma(u32x4 a, u32x4 b, f32x16 c) {
  return __builtin_amdgcn_mfma_f32_32x32x16_bf16(__builtin_bit_cast(bf16x8, a), __builtin_bit_cast(bf16x8, b), c, 0, 0, 0);
}
template <typename T> DI T lds_ld(unsigned addr) { return *(const LDS_AS T*)(size_t)addr; }
template <typename T> DI void lds_st(unsigned addr, T v) { *(LDS_AS T*)(size_t)addr = v; }
DI int crow(int i, int h) { return (i & 3) + 8 * (i >> 2) + 4 * h; }
DI unsigned wave_incl_scan(unsigned v) {
  v += __builtin_amdgcn_update_dpp(0u, v, 0x111, 0xF, 0xF, false);
  v += __builtin_amdgcn_update_dpp(0u, v, 0x112, 0xF, 0xF, false);
  v += __builtin_amdgcn_update_dpp(0u, v, 0x114, 0xF, 0xF, false);
  v += __builtin_amdgcn_update_dpp(0u, v, 0x118, 0xF, 0xF, false);
  v += __builtin_amdgcn_update_dpp(0u, v, 0x142, 0xA, 0xF, false);
  v += __builtin_amdgcn_update_dpp(0u, v, 0x143, 0xC, 0xF, false);
  return v;
}
DI unsigned wave_max_u32(unsigned v) {
  v = max(v, (unsigned)__builtin_amdgcn_update_dpp(0u, v, 0x111, 0xF, 0xF, false));
  v = max(v, (unsigned)__builtin_amdgcn_update_dpp(0u, v, 0x112, 0xF, 0xF, false));
  v = max(v, (unsigned)__builtin_amdgcn_update_dpp(0u, v, 0x114, 0xF, 0xF, false));
  v = max(v, (unsigned)__builtin_amdgcn_update_dpp(0u, v, 0x118, 0xF, 0xF, false));
  v = max(v, (unsigned)__builtin_amdgcn_update_dpp(0u, v, 0x142, 0xA, 0xF, false));
  v = max(v, (unsigned)__builtin_amdgcn_update_dpp(0u, v, 0x143, 0xC, 0xF, false));
  return v;
}
DI unsigned absbits(float f) { return __float_as_uint(f) & 0x7FFFFFFFu; }
DI void lds_max(unsigned addr, unsigned v) {
  __hip_atomic_fetch_max((LDS_AS unsigned*)(size_t)addr, v, __ATOMIC_RELAXED, __HIP_MEMORY_SCOPE_WORKGROUP);
}
DI int gstart(int g) { return (g * NN + (NG - 1)) / NG; }

constexpr int PB_T1 = 196;
constexpr int PB_T2 = PB_T1 + NCH;
constexpr int PB_WB = PB_T2 + 8;
constexpr int PB_W2 = PB_WB + 8;
constexpr int PREP_LDS = 65536 + 64;

__global__ __launch_bounds__(512) void prep_kernel(const unsigned short* __restrict__ x, const uint32_t* __restrict__ ea,
                                                   const int* __restrict__ src, const int* __restrict__ dst,
                                                   const unsigned short* __restrict__ w1, const unsigned short* __restrict__ w2,
                                                   char* __restrict__ ws) {
  __shared__ __attribute__((aligned(16))) char lds[PREP_LDS];
  const unsigned lb = (unsigned)(size_t)lds;
  const int tid = threadIdx.x, lane = tid & 63, wv = tid >> 6;
  const int blk = blockIdx.x;
  PSTAMP(0);
  if (blk < PB_T1) {
    const int n0 = (blk >> 1) * 128, half = blk & 1;
    const unsigned XO = lb, WO = lb + 32768;
    const __amdgpu_buffer_rsrc_t srdPw = __builtin_amdgcn_make_buffer_rsrc((void*)(ws + WS_P), (short)0, NN * 512, 0x00020000);
    u32x4 xv[4], wv4[4];
#pragma unroll
    for (int k = 0; k < 4; ++k) {
      const int q = tid + 512 * k, row = q >> 4, ch = q & 15;
      const int n = min(n0 + row, NN - 1);
      xv[k] = *(const u32x4*)(x + (size_t)n * 128 + ch * 8);
      wv4[k] = *(const u32x4*)(w1 + (size_t)row * 256 + half * 128 + ch * 8);
    }
#pragma unroll
    for (int k = 0; k < 4; ++k) {
      const int q = tid + 512 * k, row = q >> 4, ch = q & 15;
      lds_st<u32x4>(XO + row * 256 + ((ch ^ (row & 15)) << 4), xv[k]);
      lds_st<u32x4>(WO + row * 256 + ((ch ^ (((row & 3) << 2) | ((row >> 2) & 3))) << 4), wv4[k]);
    }
    if (tid == 0) lds_st<unsigned>(lb + 65536, 0u);
    __syncthreads();
    PSTAMP(1);
    const int nt = wv & 3, cp = wv >> 2;
    const int r = lane & 31, h = lane >> 5, bk = (lane >> 4) & 1, q4 = (lane & 15) >> 2, p4 = lane & 3;
    f32x16 acc[2];
#pragma unroll
    for (int i = 0; i < 16; ++i) { acc[0][i] = 0.f; acc[1][i] = 0.f; }
    const int xrow = 32 * nt + r;
    const unsigned xb = XO + xrow * 256 + ((h ^ (xrow & 15)) << 4);
    const int chw = 8 * cp + 2 * bk + (p4 >> 1), mA = (q4 << 2) | (2 * h), mB = mA | 1;
    const unsigned wa0 = WO + (8 * h + q4) * 256 + ((chw ^ mA) << 4) + 8 * (p4 & 1);
    const unsigned wa1 = WO + (8 * h + q4 + 4) * 256 + ((chw ^ mB) << 4) + 8 * (p4 & 1);
#pragma unroll
    for (int ks = 0; ks < 8; ++ks) {
      const u32x4 bfr = lds_ld<u32x4>(xb ^ (unsigned)(ks << 5));
#pragma unroll
      for (int ct = 0; ct < 2; ++ct) {
        const s16x4 lo = __builtin_amdgcn_ds_read_tr16_b64_v4i16((LDS_AS s16x4*)(size_t)((wa0 ^ (unsigned)(ct << 6)) + ks * 4096));
        const s16x4 hi = __builtin_amdgcn_ds_read_tr16_b64_v4i16((LDS_AS s16x4*)(size_t)((wa1 ^ (unsigned)(ct << 6)) + ks * 4096));
        const bf16x8 afr = __builtin_shufflevector(lo, hi, 0, 1, 2, 3, 4, 5, 6, 7);
        acc[ct] = mfma(__builtin_bit_cast(u32x4, afr), bfr, acc[ct]);
      }
    }
    {
      int mi = 0;
      unsigned mu = 0;
#pragma unroll
      for (int i = 0; i < 16; ++i) {
        const unsigned a = __float_as_uint(acc[0][i]), b = __float_as_uint(acc[1][i]);
        mi = max(mi, max((int)a, (int)b));
        mu = max(mu, max(a, b));
      }
      const unsigned m = wave_max_u32(max((unsigned)mi, mu & 0x7FFFFFFFu));
      if (lane == 63) lds_max(lb + 65536, m);
    }
    __syncthreads();
    PSTAMP(2);
#pragma unroll
    for (int ct = 0; ct < 2; ++ct) {
      const float sc = ct ? LOG2E : LSCALE;
#pragma unroll
      for (int g = 0; g < 4; ++g) {
        u32x2 pk;
        pk[0] = pack_bf16(acc[ct][4 * g] * sc, acc[ct][4 * g + 1] * sc);
        pk[1] = pack_bf16(acc[ct][4 * g + 2] * sc, acc[ct][4 * g + 3] * sc);
        lds_st<u32x2>(lb + xrow * 272 + (64 * cp + 32 * ct + 8 * g + 4 * h) * 2, pk);
      }
    }
    __syncthreads();
    if (tid == 0) ((unsigned*)(ws + WS_MX))[blk] = lds_ld<unsigned>(lb + 65536);
#pragma unroll
    for (int k = 0; k < 4; ++k) {
      const int q = tid + 512 * k, row = q >> 4, ch = q & 15;
      __builtin_amdgcn_raw_buffer_store_b128(lds_ld<u32x4>(lb + row * 272 + (ch << 4)), srdPw, (n0 + row) * 512 + half * 256 + ch * 16, 0, STORE_AUX);
    }
  } else if (blk < PB_T2) {
    const int bi = blk - PB_T1;
    const int b = ((blk & 7) << 5) + (bi >> 3);
    const unsigned HO = lb, SO = lb + 2048, RO = lb + 4096;
    const __amdgpu_buffer_rsrc_t srdRw = __builtin_amdgcn_make_buffer_rsrc((void*)(ws + WS_REC + (size_t)b * CHP * 12), (short)0, CHP * 12, 0x00020000);
    lds_st<unsigned>(HO + tid * 4, 0u);
    if (tid == 0) lds_st<unsigned>(SO + 128, 0u);
    int dv[4], sv[4];
    u32x2 ev[4];
#pragma unroll
    for (int k = 0; k < 4; ++k) {
      const int i = tid + 512 * k, e = b * CH + i;
      const bool ok = i < CH && e < NE;
      const int ec = ok ? e : 0;
      dv[k] = dst[ec];
      sv[k] = src[ec];
      ev[k] = *(const u32x2*)(ea + (size_t)ec * 2);
      if (!ok) dv[k] = -1;
    }
    __syncthreads();
    PSTAMP(1);
    int gk[4];
    unsigned rk[4];
#pragma unroll
    for (int k = 0; k < 4; ++k) {
      gk[k] = dv[k] >= 0 ? (int)(((unsigned)dv[k] * NG) / NN) : NG;
      rk[k] = __hip_atomic_fetch_add((LDS_AS unsigned*)(size_t)(dv[k] >= 0 ? HO + gk[k] * 4 : SO + 64), 1u, __ATOMIC_RELAXED, __HIP_MEMORY_SCOPE_WORKGROUP);
    }
    {
      u16x2 pm = {0, 0};
#pragma unroll
      for (int k = 0; k < 4; ++k) {
        pm = __builtin_elementwise_max(pm, __builtin_bit_cast(u16x2, ev[k][0] & 0x7FFF7FFFu));
        pm = __builtin_elementwise_max(pm, __builtin_bit_cast(u16x2, ev[k][1] & 0x7FFF7FFFu));
      }
      const unsigned m = wave_max_u32((unsigned)max(pm[0], pm[1]) << 16);
      if (lane == 63) lds_max(SO + 128, m);
    }
    __syncthreads();
    PSTAMP(2);
    const unsigned c = lds_ld<unsigned>(HO + tid * 4);
    const unsigned inc = wave_incl_scan(c);
    if (lane == 63) lds_st<unsigned>(SO + wv * 4, inc);
    __syncthreads();
    const u32x4 t0 = lds_ld<u32x4>(SO), t1 = lds_ld<u32x4>(SO + 16);
    unsigned pre = 0;
    pre += wv > 0 ? t0[0] : 0u; pre += wv > 1 ? t0[1] : 0u; pre += wv > 2 ? t0[2] : 0u; pre += wv > 3 ? t0[3] : 0u;
    pre += wv > 4 ? t1[0] : 0u; pre += wv > 5 ? t1[1] : 0u; pre += wv > 6 ? t1[2] : 0u;
    const unsigned start = pre + inc - c;
    ((uint32_t*)(ws + WS_TAB))[tid * NCH + b] = start | (c << 16);
    if (tid == 0) ((unsigned*)(ws + WS_MX))[256 + b] = lds_ld<unsigned>(SO + 128);
    lds_st<unsigned>(HO + tid * 4, start);
    __syncthreads();
#pragma unroll
    for (int k = 0; k < 4; ++k) {
      if (dv[k] >= 0) {
        const unsigned st = lds_ld<unsigned>(HO + gk[k] * 4);
        const unsigned ra = RO + (st + rk[k]) * 12;
        lds_st<unsigned>(ra, ((unsigned)sv[k] << 9) | ((unsigned)(dv[k] - gstart(gk[k])) << 4));
        lds_st<unsigned>(ra + 4, ev[k][0]);
        lds_st<unsigned>(ra + 8, ev[k][1]);
      }
    }
    __syncthreads();
#pragma unroll
    for (int k = 0; k < 3; ++k) {
      const int i = tid + 512 * k;
      if (i < CHP * 12 / 16) __builtin_amdgcn_raw_buffer_store_b128(lds_ld<u32x4>(RO + i * 16), srdRw, i * 16, 0, STORE_AUX);
    }
  } else if (blk < PB_WB) {
    const int q = (blk - PB_T2) * 512 + tid;
    const int l = q & 63, ks = (q >> 6) & 7, ct = q >> 9;
    const int col = 32 * ct + (l & 31), k0 = 128 + 16 * ks + 8 * (l >> 5);
    unsigned short v[8];
#pragma unroll
    for (int j = 0; j < 8; ++j) v[j] = w1[(size_t)(k0 + j) * 256 + col];
    u32x4 o;
#pragma unroll
    for (int j = 0; j < 4; ++j) o[j] = v[2 * j] | ((unsigned)v[2 * j + 1] << 16);
    *(u32x4*)(ws + WS_WB + (size_t)q * 16) = o;
  } else {
    const int q = (blk - PB_WB) * 512 + tid;
    const int l = q & 63, ot = (q >> 6) & 3, ks = q >> 8;
    const int col = 32 * ot + (l & 31), h = l >> 5;
    unsigned short v[8];
#pragma unroll
    for (int j = 0; j < 8; ++j) v[j] = w2[(size_t)(32 * (ks >> 1) + crow(8 * (ks & 1) + j, h)) * 128 + col];
    u32x4 o;
#pragma unroll
    for (int j = 0; j < 4; ++j) o[j] = v[2 * j] | ((unsigned)v[2 * j + 1] << 16);
    *(u32x4*)(ws + WS_W2 + (size_t)q * 16) = o;
  }
  PSTAMP(3);
}

constexpr unsigned L_TP = 0;
constexpr unsigned L_WORD = 32768;
constexpr unsigned WORD_TEAM = 36 * 128;
constexpr unsigned L_MASK = L_WORD + 2 * WORD_TEAM;
constexpr unsigned MASK_TEAM = 34 * 128;
constexpr unsigned L_CNT = L_MASK + 2 * MASK_TEAM;
constexpr unsigned L_TOT = L_CNT + 256;
constexpr unsigned L_MAX = L_TOT + 16;
constexpr unsigned L_PROG = L_TOT + 32;
constexpr unsigned L_BKT = L_TOT + 64;
constexpr unsigned L_ZEND = L_TOT + 128;
constexpr unsigned L_ALUT = (L_ZEND + 511) / 512 * 512;
constexpr unsigned L_BLUT = L_ALUT + 2048;
constexpr unsigned L_EA = L_BLUT + 4096;
constexpr unsigned L_AEND = L_EA + 4096;
constexpr unsigned L_TN = 65536;
constexpr unsigned L_STG = 98304;
constexpr unsigned L_END = 163840;
static_assert(L_AEND <= 65536 && (L_ZEND - L_MASK) % 16 == 0 && L_ALUT % 512 == 0, "LDS region A");
constexpr unsigned PADWORD = 0x7FFFFE00u | (31u << 4);

__global__ __launch_bounds__(512, 2) void main_kernel(const unsigned short* __restrict__ x, const unsigned short* __restrict__ w1,
                                                      const unsigned short* __restrict__ b1, const unsigned short* __restrict__ b2,
                                                      char* __restrict__ ws, unsigned short* __restrict__ out) {
  __shared__ __attribute__((aligned(16))) char lds[L_END];
  asm volatile("" ::"s"((unsigned)(size_t)lds));
  const int tid = threadIdx.x, lane = tid & 63;
  const int wv = __builtin_amdgcn_readfirstlane(tid >> 6);
  const int team = wv >> 2, w = wv & 3, tt = tid & 255;
  const int wg = (blockIdx.x & 7) * 32 + (blockIdx.x >> 3);
  const int g = 2 * wg + team;
  const int n0 = gstart(g), G = gstart(g + 1) - n0;
  const int r = lane & 31, h = lane >> 5;

  const __amdgpu_buffer_rsrc_t srdP = __builtin_amdgcn_make_buffer_rsrc((void*)(ws + WS_P - 3072), (short)0, NN * 512 + 3072, 0x00020000);
  STAMP(0);
  const uint32_t tabv = ((const uint32_t*)(ws + WS_TAB))[g * NCH + tt];
  u32x4 xs[2];
#pragma unroll
  for (int k = 0; k < 2; ++k) {
    const int q = tt + 256 * k, row = q >> 4, ch = q & 15;
    const int n = min(n0 + row, NN - 1);
    xs[k] = *(const u32x4*)(x + (size_t)n * 128 + ch * 8);
  }
  unsigned mxv = ((const unsigned*)(ws + WS_MX))[tid];
  const unsigned short cb0 = b1[32 * wv + r];
  const unsigned short cb1 = w1[(size_t)(256 + 2 * h) * 256 + 32 * wv + r];
  const unsigned short cb2 = w1[(size_t)(257 + 2 * h) * 256 + 32 * wv + r];

  STAMP(1);
  {
    const u32x4 pad = {PADWORD, PADWORD, PADWORD, PADWORD}, zero = {0, 0, 0, 0};
    lds_st<u32x4>(L_WORD + tid * 16, pad);
    if (tid < (int)(2 * WORD_TEAM / 16 - 512)) lds_st<u32x4>(L_WORD + (512 + tid) * 16, pad);
    lds_st<u32x4>(L_MASK + tid * 16, zero);
    if (tid < (int)((L_ZEND - L_MASK) / 16 - 512)) lds_st<u32x4>(L_MASK + (512 + tid) * 16, zero);
  }
  if (tid < 128) {
    const int u = tid & 31, hh = (tid >> 5) & 1, s = tid >> 6;
    const int rho = u < 26 ? u : u + 2;
    const int i = (rho & 3) + 4 * (rho >> 3), j = i & 7;
    const bool on = u < 28 && ((rho >> 2) & 1) == hh && (i >> 3) == s;
    const unsigned one = on ? (0x3F80u << (16 * (j & 1))) : 0u;
    u32x4 v;
#pragma unroll
    for (int q = 0; q < 4; ++q) v[q] = (j >> 1) == q ? one : 0u;
    lds_st<u32x4>(L_ALUT + tid * 16, v);
  } else if (tid < 384) {
    const int bb = tid - 128;
    u32x4 v;
#pragma unroll
    for (int j = 0; j < 4; ++j) v[j] = ((bb >> (2 * j)) & 1 ? 0x3F80u : 0u) | ((bb >> (2 * j + 1)) & 1 ? 0x3F800000u : 0u);
    lds_st<u32x4>(L_BLUT + bb * 16, v);
  }
  STAMP(2);
  u32x4 wb[8];
#pragma unroll
  for (int k = 0; k < 8; ++k) wb[k] = *(const u32x4*)(ws + WS_WB + (size_t)(wv * 8 + k) * 1024 + lane * 16);

  const unsigned wbase = L_WORD + team * WORD_TEAM;
  const unsigned stg = L_STG + wv * 8192;
  const unsigned lc0 = (unsigned)(w * 128 + (((lane & 7) ^ ((lane >> 4) & 3)) << 4));
  const unsigned lc1 = lc0 ^ 64u;
  const unsigned w4a = wbase + (lane >> 3) * 16;
  const unsigned eav = (unsigned)((2 * r + h) * 4);
  STAMP(3);

  auto xaddr = [&](int tm, int row, int ch) { return L_STG + (unsigned)(2 * tm + (row >> 4)) * 8192 + (unsigned)(row & 15) * 256 + (unsigned)((ch ^ (row & 15)) << 4); };
  auto qaddr = [&](int tm, int ct, int s) { const int i = (tm * 8 + ct) * 2 + s; return L_STG + (unsigned)(i >> 2) * 8192 + 4096 + (unsigned)(i & 3) * 1024 + lane * 16; };
  const unsigned IDX = L_STG + (4 + team) * 8192;
#pragma unroll
  for (int k = 0; k < 2; ++k) {
    const int q = tt + 256 * k, row = q >> 4, ch = q & 15;
    u32x4 v = xs[k];
    if (row >= G) v = u32x4{0, 0, 0, 0};
    lds_st<u32x4>(xaddr(team, row, ch), v);
  }
  STAMP(4);
  __syncthreads();

  STAMP(5);
  const unsigned segs = tabv & 0xFFFFu, segc = tabv >> 16;
  const unsigned inc = wave_incl_scan(segc);
  unsigned wbase0 = 0;
  if (lane == 63) wbase0 = __hip_atomic_fetch_add((LDS_AS unsigned*)(size_t)(L_TOT + team * 4), inc, __ATOMIC_RELAXED, __HIP_MEMORY_SCOPE_WORKGROUP);
  const unsigned pos0 = __builtin_amdgcn_readlane(wbase0, 63) + inc - segc;
  const unsigned rec0 = (unsigned)tt * CHP + segs;
  char* eap = ws + WS_EA + (size_t)g * CAP * 8;
  {
    const unsigned kend = pos0 < (unsigned)CAP ? min(segc, (unsigned)CAP - pos0) : 0u;
    for (unsigned k = 0; k < kend; ++k) lds_st<unsigned>(IDX + (pos0 + k) * 4, rec0 + k);
  }
  STAMP(6);
  __syncthreads();

  unsigned total;
  int nrounds;
  {
    const unsigned t0 = lds_ld<unsigned>(L_TOT), t1 = lds_ld<unsigned>(L_TOT + 4);
    total = __builtin_amdgcn_readfirstlane(team ? t1 : t0);
    nrounds = __builtin_amdgcn_readfirstlane((int)((max(t0, t1) + CAP - 1) / CAP));
  }
  STAMP(7);
  u32x3 rec[4];
  auto fetch_records = [&](unsigned rem) {
#pragma unroll
    for (int k = 0; k < 4; ++k) {
      const unsigned p = tt + 256 * k;
      const unsigned ri = lds_ld<unsigned>(IDX + p * 4);
      rec[k] = *(const u32x3_a4*)(ws + WS_REC + (size_t)(p < rem ? ri : 0u) * 12);
    }
  };
  fetch_records(min(total, (unsigned)CAP));
  STAMP(8);
  {
    const int u = r < 26 ? r : (r == 28 || r == 29) ? r - 2 : 31;
    const float sc = (wv & 1) ? LOG2E : LSCALE;
    const float cin0 = bf2f(cb0), cin1 = bf2f(cb1), cin2 = bf2f(cb2);
    f32x16 acc[2];
    u32x4 af[2][8];
#pragma unroll
    for (int tm = 0; tm < 2; ++tm) {
#pragma unroll
      for (int ks = 0; ks < 8; ++ks) af[tm][ks] = lds_ld<u32x4>(xaddr(tm, u, 2 * ks + h));
#pragma unroll
      for (int i = 0; i < 14; ++i) acc[tm][i] = cin0;
      acc[tm][14] = cin1;
      acc[tm][15] = cin2;
    }
    __builtin_amdgcn_sched_barrier(0);
#pragma unroll
    for (int ks = 0; ks < 8; ++ks) {
      acc[0] = mfma(af[0][ks], wb[ks], acc[0]);
      acc[1] = mfma(af[1][ks], wb[ks], acc[1]);
    }
    int mi = 0;
    unsigned mu = 0;
#pragma unroll
    for (int tm = 0; tm < 2; ++tm) {
#pragma unroll
      for (int i = 0; i < 14; i += 2) {
        const unsigned a = __float_as_uint(acc[tm][i]), b = __float_as_uint(acc[tm][i + 1]);
        mi = max(mi, max((int)a, (int)b));
        mu = max(mu, max(a, b));
      }
#pragma unroll
      for (int s = 0; s < 2; ++s) {
        u32x4 v;
#pragma unroll
        for (int j = 0; j < 4; ++j) v[j] = pack_bf16(acc[tm][8 * s + 2 * j] * sc, acc[tm][8 * s + 2 * j + 1] * sc);
        lds_st<u32x4>(qaddr(tm, wv, s), v);
      }
    }
    const unsigned mq = wave_max_u32(max((unsigned)mi, mu & 0x7FFFFFFFu));
    const unsigned mw = wave_max_u32(max(absbits(cin1), absbits(cin2)));
    const unsigned mx = wave_max_u32(tid < PB_T1 || tid >= 256 ? mxv : 0u);
    if (lane == 63) { lds_max(L_MAX + (team ? 4 : 0), mx); lds_max(L_MAX + 8, mq); lds_max(L_MAX + 12, mw); }
  }
  STAMP(9);
  f32x16 S[2];
#pragma unroll
  for (int i = 0; i < 16; ++i) { S[0][i] = 0.f; S[1][i] = 0.f; }
  auto place_at = [&](unsigned p, const u32x3& rc) {
    const unsigned tile = p >> 5, rr = p & 31;
    lds_st<unsigned>(wbase + tile * 128 + ((rr & 7) * 4 + (rr >> 3)) * 4, rc[0]);
    const unsigned u = (rc[0] >> 4) & 31;
    const unsigned bit = 16 * (rr >> 4) + 8 * ((rr >> 2) & 1) + 4 * ((rr >> 3) & 1) + (rr & 3);
    __hip_atomic_fetch_or((LDS_AS unsigned*)(size_t)(L_MASK + team * MASK_TEAM + tile * 128 + u * 4), 1u << bit, __ATOMIC_RELAXED, __HIP_MEMORY_SCOPE_WORKGROUP);
    __hip_atomic_fetch_add((LDS_AS unsigned*)(size_t)(L_CNT + team * 128 + u * 4), 1u, __ATOMIC_RELAXED, __HIP_MEMORY_SCOPE_WORKGROUP);
    *(u32x2*)(eap + (size_t)p * 8) = u32x2{rc[1], rc[2]};
  };
  const unsigned rem0 = min(total, (unsigned)CAP);
  {
    unsigned bk[4], rk[4];
#pragma unroll
    for (int k = 0; k < 4; ++k) {
      bk[k] = ((rec[k][0] >> 9) * 8u) / (unsigned)NN;
      rk[k] = 0;
      if (tt + 256u * k < rem0) rk[k] = __hip_atomic_fetch_add((LDS_AS unsigned*)(size_t)(L_BKT + team * 32 + bk[k] * 4), 1u, __ATOMIC_RELAXED, __HIP_MEMORY_SCOPE_WORKGROUP);
    }
    asm volatile("s_waitcnt lgkmcnt(0)" ::: "memory");
    __builtin_amdgcn_s_barrier();
    const unsigned cnt8 = lds_ld<unsigned>(L_BKT + team * 32 + (lane & 7) * 4);
    const unsigned pre8 = wave_incl_scan(lane < 8 ? cnt8 : 0u) - cnt8;
#pragma unroll
    for (int k = 0; k < 4; ++k) {
      const unsigned base = __builtin_amdgcn_ds_bpermute(bk[k] * 4, pre8);
      if (tt + 256u * k < rem0) place_at(base + rk[k], rec[k]);
    }
  }
  STAMP(10);
  asm volatile("s_waitcnt lgkmcnt(0)" ::: "memory");
  __builtin_amdgcn_s_barrier();
  STAMP(11);

  auto dmaP = [&](int B, u32x4 wd) {
    const unsigned v0 = (wd[0] & 0xFFFFFE00u) | lc0, v1 = (wd[1] & 0xFFFFFE00u) | lc1;
    const unsigned v2 = (wd[2] & 0xFFFFFE00u) | lc0, v3 = (wd[3] & 0xFFFFFE00u) | lc1;
    const unsigned m0p = stg + B * 4096;
    asm volatile(
        "s_mov_b32 m0, %4\n\ts_nop 0\n\tbuffer_load_dwordx4 %0, %5, %6 offen lds\n\t"
        "buffer_load_dwordx4 %1, %5, %7 offen offset:1024 lds\n\t"
        "buffer_load_dwordx4 %2, %5, %8 offen offset:2048 lds\n\t"
        "buffer_load_dwordx4 %3, %5, 0 offen offset:3072 lds"
        ::"v"(v0), "v"(v1), "v"(v2), "v"(v3), "s"(m0p), "s"(srdP), "s"(3072), "s"(2048), "s"(1024)
        : "memory");
  };
  auto dmaE = [&](int T, int B, __amdgpu_buffer_rsrc_t srdE) {
    const unsigned ve = eav + (unsigned)T * 256u;
    const unsigned m0e = L_EA + wv * 512 + B * 256;
    asm volatile("s_mov_b32 m0, %1\n\ts_nop 0\n\tbuffer_load_dword %0, %2, 0 offen lds" ::"v"(ve), "s"(m0e), "s"(srdE) : "memory");
  };
  const __amdgpu_buffer_rsrc_t srdE0 = __builtin_amdgcn_make_buffer_rsrc((void*)eap, (short)0, (int)(rem0 * 8), 0x00020000);
  dmaP(0, lds_ld<u32x4>(w4a));

  u32x4 qf[2][2];
#pragma unroll
  for (int t = 0; t < 2; ++t)
#pragma unroll
    for (int s = 0; s < 2; ++s) qf[t][s] = lds_ld<u32x4>(qaddr(team, 2 * w + t, s));
  bool fast;
  {
    const u32x4 mx = lds_ld<u32x4>(L_MAX);
    const float bound = __uint_as_float(mx[0]) + __uint_as_float(mx[2]) + 4.0f * __uint_as_float(mx[1]) * __uint_as_float(mx[3]);
    fast = __builtin_amdgcn_readfirstlane(bound < 120.0f ? 1 : 0) != 0 && nrounds <= 1;
  }
  asm volatile("" ::"v"(qf[0][0]), "v"(qf[0][1]), "v"(qf[1][0]), "v"(qf[1][1]));
  STAMP(13);
  constexpr unsigned TINY = 110u * 128u;
#pragma unroll
  for (int k = 0; k < 4; ++k) {
    const unsigned c0 = (unsigned)(k * 512 + tid) * 8;
    if (k < 3 || c0 < TINY) {
      u32x4 tp;
#pragma unroll
      for (int j = 0; j < 4; ++j) {
        const unsigned b0 = ((c0 + 2 * j) << 16) | 0x8000u;
        const f32x2_t v = f32x2_t{__uint_as_float(b0), __uint_as_float(b0 + 0x10000u)} * 32.0f;
        tp[j] = pack_bf16(v[0], v[1]);
      }
      lds_st<u32x4>(L_TP + c0 * 2, tp);
      lds_st<u32x4>(L_TN + c0 * 2, tp | 0x80008000u);
    }
  }
  for (unsigned c0 = TINY + tid * 4; c0 < 16384u; c0 += 2048u) {
    u32x2 tp, tn;
#pragma unroll
    for (int j = 0; j < 2; ++j) {
      const unsigned b0 = ((c0 + 2 * j) << 16) | 0x8000u;
      const float za = __uint_as_float(b0) * 64.0f, zb = __uint_as_float(b0 + 0x10000u) * 64.0f;
      const float ea_ = __builtin_amdgcn_exp2f(-LOG2E * za), eb_ = __builtin_amdgcn_exp2f(-LOG2E * zb);
      const float ra = __builtin_amdgcn_rcpf(1.0f + ea_), rb = __builtin_amdgcn_rcpf(1.0f + eb_);
      tp[j] = pack_bf16(za * ra, zb * rb);
      tn[j] = pack_bf16(-(za * ea_) * ra, -(zb * eb_) * rb);
    }
    lds_st<u32x2>(L_TP + c0 * 2, tp);
    lds_st<u32x2>(L_TN + c0 * 2, tn);
  }
  STAMP(19);
  u32x4 idn[2];
#pragma unroll
  for (int s = 0; s < 2; ++s) {
    const unsigned d = (unsigned)(r - 16 * s - 8 * h);
    idn[s] = lds_ld<u32x4>(L_BLUT + (d < 8u ? (16u << d) : 0u));
  }
  const unsigned pb = (unsigned)(r * 128 + ((h ^ ((r >> 1) & 7)) << 4));
  unsigned pa0 = stg + pb, pa1 = stg + (pb ^ 32u), pa2 = stg + (pb ^ 64u), pa3 = stg + (pb ^ 96u);
  unsigned w1a = wbase + ((r & 7) * 4 + (r >> 3)) * 4;
  unsigned mka = L_MASK + team * MASK_TEAM + r * 4 + h;
  unsigned eal = L_EA + wv * 512 + lane * 4;
  unsigned alh = L_ALUT + h * 512;
  asm volatile("" : "+v"(idn[0]), "+v"(idn[1]), "+v"(pa0), "+v"(pa1), "+v"(pa2), "+v"(pa3), "+v"(w1a), "+v"(mka), "+v"(eal), "+v"(alh));
  {
    const u32x4 wd_1 = lds_ld<u32x4>(w4a + 128);
    STAMP(20);
    asm volatile("s_waitcnt vmcnt(4) lgkmcnt(0)" ::: "memory");
    STAMP(21);
    __builtin_amdgcn_s_barrier();
    dmaE(0, 0, srdE0);
    dmaP(1, wd_1);
    dmaE(1, 1, srdE0);
  }
  STAMP(12);
  auto load_ops = [&](unsigned wd, int B, u32x4 (&pp)[4], u32x4 (&aa)[2]) {
    const unsigned ad = (wd & 0x1F0u) | alh;
    aa[0] = lds_ld<u32x4>(ad);
    const u32x3 a1 = lds_ld<u32x3>(ad + 1024);
    aa[1][0] = a1[0]; aa[1][1] = a1[1]; aa[1][2] = a1[2];
    aa[1][3] = lds_ld<unsigned>(eal + B * 256);
    pp[0] = lds_ld<u32x4>(pa0 + B * 4096);
    pp[1] = lds_ld<u32x4>(pa1 + B * 4096);
    pp[2] = lds_ld<u32x4>(pa2 + B * 4096);
    pp[3] = lds_ld<u32x4>(pa3 + B * 4096);
  };
#define SB() __builtin_amdgcn_sched_barrier(0)
#define KEEP_OPS() asm volatile("" ::"v"(pp[0]), "v"(pp[1]), "v"(pp[2]), "v"(pp[3]), "v"(aa[1]))
  f32x16 zz;
#pragma unroll
  for (int i = 0; i < 16; ++i) zz[i] = 0.f;

  if (fast) {
    const int ntiles = (int)((rem0 + 31) >> 5);
    if (ntiles > 0) {
      u32x4 pp[4], aa[2];
      asm volatile("s_waitcnt vmcnt(5)" ::: "memory");
      STAMP(14);
      load_ops(lds_ld<unsigned>(w1a), 0, pp, aa);
      {
        const u32x4 wd = lds_ld<u32x4>(w4a + 256);
        KEEP_OPS();
        dmaP(0, wd);
        dmaE(2, 0, srdE0);
      }
      STAMP(15);
      CSTAMP(30);
      u32x4 bp[2], bn[2], h0[2], h1[2], wd4;
      f32x16 z0, z1, z1p;
      unsigned lk[16], mb0, mb1, wd1, orem;
      f32x2_t E[8], R[8], M[8];
#pragma unroll
      for (int i = 0; i < 16; ++i) { z1p[i] = 0.f; lk[i] = 0; }
      bp[0] = u32x4{0, 0, 0, 0};
      bp[1] = bp[0];
#define VE(j) E[j] = f32x2_t{__builtin_amdgcn_exp2f(-z1p[2 * (j)]), __builtin_amdgcn_exp2f(-z1p[2 * (j) + 1])}
#define VA(j) E[j] = E[j] + 1.0f
#define VR(j) R[j] = f32x2_t{__builtin_amdgcn_rcpf(E[j][0]), __builtin_amdgcn_rcpf(E[j][1])}
#define VM(j) M[j] = f32x2_t{z1p[2 * (j)], z1p[2 * (j) + 1]} * R[j]
#define VC(j) h1[(j) >> 2][(j) & 3] = pack_bf16(M[j][0], M[j][1])
#define MG(j) h0[(j) >> 2][(j) & 3] = lk[2 * (j)] | (lk[2 * (j) + 1] << 16)
#define LL(i) lk[i] = lds_ld<unsigned short>((__float_as_uint(z0[i]) >> 16) << 1)
      auto partA = [&](int T) {
        z0 = mfma(aa[0], qf[0][0], zz); VE(0); MG(0); SB();
        z0 = mfma(aa[1], qf[0][1], z0); VE(1); VA(0); MG(1); SB();
        z0 = mfma(pp[0], idn[0], z0); VE(2); VA(1); VR(0); MG(2);
        mb0 = lds_ld<unsigned char>(mka + T * 128); mb1 = lds_ld<unsigned char>(mka + T * 128 + 2);
        wd1 = lds_ld<unsigned>(w1a + (T + 1) * 128); wd4 = lds_ld<u32x4>(w4a + (T + 3) * 128);
        SB();
        z0 = mfma(pp[1], idn[1], z0); VE(3); VA(2); VR(1); VM(0); MG(3); SB();
        z1 = mfma(aa[0], qf[1][0], zz); VE(4); VA(3); VR(2); VM(1); VC(0); MG(4); SB();
      };
      auto partB = [&](int T, auto BC) {
        constexpr int B = decltype(BC)::value;
        u32x4 pn[4], an[2];
        asm volatile("s_waitcnt vmcnt(5)" ::: "memory");
        load_ops(wd1, B, pn, an);
        z1 = mfma(aa[1], qf[1][1], z1); VE(5); VA(4); VR(3); VM(2); VC(1); MG(5); LL(0); LL(1); SB();
        z1 = mfma(pp[2], idn[0], z1); VE(6); VA(5); VR(4); VM(3); VC(2); MG(6); LL(2); LL(3); SB();
        z1 = mfma(pp[3], idn[1], z1); VE(7); VA(6); VR(5); VM(4); VC(3); MG(7); LL(4); LL(5);
        asm volatile("" ::"v"(pn[0]), "v"(pn[1]), "v"(pn[2]), "v"(pn[3]), "v"(an[1]));
        dmaP(B, wd4);
        dmaE(T + 3, B, srdE0);
        SB();
        S[0] = mfma(h0[0], bp[0], S[0]); VA(7); VR(6); VM(5); VC(4); LL(6); LL(7); LL(8); SB();
        S[0] = mfma(h0[1], bp[1], S[0]); VR(7); VM(6); VC(5); LL(9); LL(10); LL(11);
        bn[0] = lds_ld<u32x4>(L_BLUT + mb0 * 16); bn[1] = lds_ld<u32x4>(L_BLUT + mb1 * 16);
        SB();
        S[1] = mfma(h1[0], bp[0], S[1]); VM(7); VC(6); LL(12); LL(13); LL(14); SB();
        VC(7); LL(15);
        S[1] = mfma(h1[1], bp[1], S[1]);
        SB();
        z1p = z1;
        bp[0] = bn[0];
        bp[1] = bn[1];
        pp[0] = pn[0]; pp[1] = pn[1]; pp[2] = pn[2]; pp[3] = pn[3];
        aa[0] = an[0]; aa[1] = an[1];
      };
      const std::integral_constant<int, 0> B0;
      const std::integral_constant<int, 1> B1;
      partA(0);
      int t = 0;
      for (; t + 2 < ntiles; t += 2) {
        partB(t, B1);
        partA(t + 1);
        partB(t + 1, B0);
        partA(t + 2);
      }
      partB(t, B1);
      if (t + 1 < ntiles) {
        partA(t + 1);
        partB(t + 1, B0);
      }
#pragma unroll
      for (int j = 0; j < 8; ++j) { VE(j); VA(j); VR(j); VM(j); VC(j); MG(j); }
      S[0] = mfma(h0[0], bp[0], S[0]);
      S[1] = mfma(h1[0], bp[0], S[1]);
      S[0] = mfma(h0[1], bp[1], S[0]);
      S[1] = mfma(h1[1], bp[1], S[1]);
    }
    asm volatile("s_waitcnt vmcnt(0)" ::: "memory");
  } else {
    int round = 0;
    do {
      const unsigned lo = (unsigned)round * CAP;
      const unsigned rem = total > lo ? min(total - lo, (unsigned)CAP) : 0u;
      const __amdgpu_buffer_rsrc_t srdE = __builtin_amdgcn_make_buffer_rsrc((void*)eap, (short)0, (int)(rem * 8), 0x00020000);
      if (round > 0) {
        __syncthreads();
        for (int i = tt; i < 36 * 32; i += 256) lds_st<unsigned>(wbase + i * 4, PADWORD);
        for (int i = tt; i < 34 * 32; i += 256) lds_st<unsigned>(L_MASK + team * MASK_TEAM + i * 4, 0u);
        for (unsigned k = 0; k < segc; ++k) {
          const unsigned p = pos0 + k - lo;
          if (p < (unsigned)CAP) lds_st<unsigned>(IDX + p * 4, rec0 + k);
        }
        __syncthreads();
        fetch_records(rem);
#pragma unroll
        for (int k = 0; k < 4; ++k)
          if (tt + 256u * k < rem) place_at(tt + 256u * k, rec[k]);
        asm volatile("s_waitcnt vmcnt(0)" ::: "memory");
        __syncthreads();
        dmaP(0, lds_ld<u32x4>(w4a));
        dmaE(0, 0, srdE);
        dmaP(1, lds_ld<u32x4>(w4a + 128));
        dmaE(1, 1, srdE);
      }
      const int ntiles = (int)((rem + 31) >> 5);
      if (ntiles > 0) {
        u32x4 pp[4], aa[2];
        asm volatile("s_waitcnt vmcnt(5)" ::: "memory");
        load_ops(lds_ld<unsigned>(w1a), 0, pp, aa);
        {
          const u32x4 wd = lds_ld<u32x4>(w4a + 256);
          KEEP_OPS();
          dmaP(0, wd);
          dmaE(2, 0, srdE);
        }
        for (int t = 0; t < ntiles; ++t) {
          const int B = (t + 1) & 1;
          f32x16 z0 = mfma(aa[0], qf[0][0], zz), z1 = mfma(aa[0], qf[1][0], zz);
          z0 = mfma(aa[1], qf[0][1], z0);
          z1 = mfma(aa[1], qf[1][1], z1);
          z0 = mfma(pp[0], idn[0], z0);
          z1 = mfma(pp[2], idn[0], z1);
          z0 = mfma(pp[1], idn[1], z0);
          z1 = mfma(pp[3], idn[1], z1);
          u32x4 bb[2], h0[2], h1[2];
          bb[0] = lds_ld<u32x4>(L_BLUT + lds_ld<unsigned char>(mka + t * 128) * 16);
          bb[1] = lds_ld<u32x4>(L_BLUT + lds_ld<unsigned char>(mka + t * 128 + 2) * 16);
#pragma unroll
          for (int j = 0; j < 8; ++j) {
            const float za = z0[2 * j] * 64.0f, zb = z0[2 * j + 1] * 64.0f, ya = z1[2 * j], yb = z1[2 * j + 1];
            h0[j >> 2][j & 3] = pack_bf16(za * __builtin_amdgcn_rcpf(1.0f + __builtin_amdgcn_exp2f(-LOG2E * za)), zb * __builtin_amdgcn_rcpf(1.0f + __builtin_amdgcn_exp2f(-LOG2E * zb)));
            h1[j >> 2][j & 3] = pack_bf16(ya * __builtin_amdgcn_rcpf(1.0f + __builtin_amdgcn_exp2f(-ya)), yb * __builtin_amdgcn_rcpf(1.0f + __builtin_amdgcn_exp2f(-yb)));
          }
          S[0] = mfma(h0[0], bb[0], S[0]);
          S[1] = mfma(h1[0], bb[0], S[1]);
          S[0] = mfma(h0[1], bb[1], S[0]);
          S[1] = mfma(h1[1], bb[1], S[1]);
          asm volatile("s_waitcnt vmcnt(5)" ::: "memory");
          const u32x4 wd = lds_ld<u32x4>(w4a + (t + 3) * 128);
          load_ops(lds_ld<unsigned>(w1a + (t + 1) * 128), B, pp, aa);
          KEEP_OPS();
          dmaP(B, wd);
          dmaE(t + 3, B, srdE);
        }
      }
      asm volatile("s_waitcnt vmcnt(0)" ::: "memory");
    } while (++round < nrounds);
  }

  STAMP(16);
  CSTAMP(31);
  u32x4 w2f[16];
#pragma unroll
  for (int ks = 0; ks < 16; ++ks) w2f[ks] = *(const u32x4*)(ws + WS_W2 + (size_t)(ks * 4 + w) * 1024 + lane * 16);
  u32x2 b2v[4];
#pragma unroll
  for (int gq = 0; gq < 4; ++gq) b2v[gq] = *(const u32x2*)(b2 + 32 * w + 8 * gq + 4 * h);
  __syncthreads();
  const unsigned EX = L_STG + team * 32768;
#pragma unroll
  for (int t = 0; t < 2; ++t) {
    const float sc = t ? LN2 : 1.0f;
#pragma unroll
    for (int s = 0; s < 2; ++s) {
      u32x4 v;
#pragma unroll
      for (int j = 0; j < 4; ++j) v[j] = pack_bf16(S[t][8 * s + 2 * j] * sc, S[t][8 * s + 2 * j + 1] * sc);
      lds_st<u32x4>(EX + (4 * w + 2 * t + s) * 1024 + lane * 16, v);
    }
  }
  __syncthreads();
  STAMP(17);
  const float deg = (float)lds_ld<unsigned>(L_CNT + team * 128 + r * 4);
  f32x16 o;
#pragma unroll
  for (int gq = 0; gq < 4; ++gq) {
    o[4 * gq + 0] = deg * __uint_as_float(b2v[gq][0] << 16);
    o[4 * gq + 1] = deg * __uint_as_float(b2v[gq][0] & 0xFFFF0000u);
    o[4 * gq + 2] = deg * __uint_as_float(b2v[gq][1] << 16);
    o[4 * gq + 3] = deg * __uint_as_float(b2v[gq][1] & 0xFFFF0000u);
  }
#pragma unroll
  for (int ks = 0; ks < 16; ++ks) o = mfma(w2f[ks], lds_ld<u32x4>(EX + ks * 1024 + lane * 16), o);
  if (r < G) {
    unsigned short* op = out + (size_t)(n0 + r) * 128 + 32 * w + 4 * h;
#pragma unroll
    for (int gq = 0; gq < 4; ++gq) {
      u32x2 v;
      v[0] = pack_bf16(o[4 * gq], o[4 * gq + 1]);
      v[1] = pack_bf16(o[4 * gq + 2], o[4 * gq + 3]);
      *(u32x2*)(op + 8 * gq) = v;
    }
  }
  STAMP(18);
}

}

extern "C" void kernel_launch(void* const* d_in, const int* in_sizes, int n_in, void* d_out, int out_size, void* d_ws,
                              size_t ws_size, hipStream_t stream) {
  const unsigned short* x = (const unsigned short*)d_in[0];
  const uint32_t* ea = (const uint32_t*)d_in[1];
  const int* src = (const int*)d_in[2];
  const int* dst = (const int*)d_in[3];
  const unsigned short* w1 = (const unsigned short*)d_in[4];
  const unsigned short* b1 = (const unsigned short*)d_in[5];
  const unsigned short* w2 = (const unsigned short*)d_in[6];
  const unsigned short* b2 = (const unsigned short*)d_in[7];
  (void)in_sizes; (void)n_in; (void)out_size; (void)ws_size;
  prep_kernel<<<PB_W2, 512, 0, stream>>>(x, ea, src, dst, w1, w2, (char*)d_ws);
  main_kernel<<<MAIN_GRID, 512, 0, stream>>>(x, w1, b1, b2, (char*)d_ws, (unsigned short*)d_out);
}
```

```cpp
#include <hip/hip_runtime.h>
#include <hip/hip_bf16.h>
#include <stdint.h>
#include <type_traits>

#define ABL 0
#define MAIN_GRID 256
#define STORE_AUX 16
#define RPREF 0
#define PREF 0
#define PREF_LINES 196
#define PRIO 1
#define PRIO_RHO 0.38f
namespace {

constexpr int NN = 12500;
constexpr int NE = 400000;
constexpr int NG = 512;
constexpr int NCH = 256;
constexpr int CH = 1563;
constexpr int CHP = 1568;
constexpr int CAP = 1024;

constexpr size_t WS_P = 4096;
constexpr size_t WS_REC = WS_P + 6400000;
constexpr size_t WS_TAB = WS_REC + (size_t)NCH * CHP * 12;
constexpr size_t WS_WB = WS_TAB + (size_t)NCH * NG * 4;
constexpr size_t WS_W2 = WS_WB + 65536;
constexpr size_t WS_TBL = WS_W2 + 65536;
constexpr size_t WS_MX = WS_TBL + 65536;
constexpr size_t WS_EA = WS_MX + 2048;
constexpr size_t WS_END = WS_EA + (size_t)NG * CAP * 8;
#define STAMP(k) do { } while (0)
#define CSTAMP(k) do { } while (0)
#define PSTAMP(k) do { } while (0)

constexpr float LOG2E = 1.4426950408889634f;
constexpr float LN2 = 0.6931471805599453f;
constexpr float LSCALE = 1.0f / 64.0f;

typedef __attribute__((ext_vector_type(8))) short bf16x8;
typedef __attribute__((ext_vector_type(4))) short s16x4;
typedef __attribute__((ext_vector_type(16))) float f32x16;
typedef __attribute__((ext_vector_type(4))) unsigned u32x4;
typedef __attribute__((ext_vector_type(2))) unsigned u32x2;
typedef __attribute__((ext_vector_type(3))) unsigned u32x3;
typedef __attribute__((ext_vector_type(3), aligned(4))) unsigned u32x3_a4;
typedef __attribute__((ext_vector_type(2))) unsigned short u16x2;
typedef __bf16 bf16x2_t __attribute__((ext_vector_type(2)));
typedef float f32x2_t __attribute__((ext_vector_type(2)));
#define LDS_AS __attribute__((address_space(3)))
#define DI __device__ __forceinline__

DI unsigned pack_bf16(float a, float b) {
  f32x2_t v = {a, b};
  bf16x2_t r = __builtin_convertvector(v, bf16x2_t);
  return __builtin_bit_cast(unsigned, r);
}
DI float bf2f(unsigned short u) { return __uint_as_float((unsigned)u << 16); }
DI f32x16 mfma(u32x4 a, u32x4 b, f32x16 c) {
  return __builtin_amdgcn_mfma_f32_32x32x16_bf16(__builtin_bit_cast(bf16x8, a), __builtin_bit_cast(bf16x8, b), c, 0, 0, 0);
}
template <typename T> DI T lds_ld(unsigned addr) { return *(const LDS_AS T*)(size_t)addr; }
template <typename T> DI void lds_st(unsigned addr, T v) { *(LDS_AS T*)(size_t)addr = v; }
DI int crow(int i, int h) { return (i & 3) + 8 * (i >> 2) + 4 * h; }
DI unsigned wave_incl_scan(unsigned v) {
  v += __builtin_amdgcn_update_dpp(0u, v, 0x111, 0xF, 0xF, false);
  v += __builtin_amdgcn_update_dpp(0u, v, 0x112, 0xF, 0xF, false);
  v += __builtin_amdgcn_update_dpp(0u, v, 0x114, 0xF, 0xF, false);
  v += __builtin_amdgcn_update_dpp(0u, v, 0x118, 0xF, 0xF, false);
  v += __builtin_amdgcn_update_dpp(0u, v, 0x142, 0xA, 0xF, false);
  v += __builtin_amdgcn_update_dpp(0u, v, 0x143, 0xC, 0xF, false);
  return v;
}
DI unsigned wave_max_u32(unsigned v) {
  v = max(v, (unsigned)__builtin_amdgcn_update_dpp(0u, v, 0x111, 0xF, 0xF, false));
  v = max(v, (unsigned)__builtin_amdgcn_update_dpp(0u, v, 0x112, 0xF, 0xF, false));
  v = max(v, (unsigned)__builtin_amdgcn_update_dpp(0u, v, 0x114, 0xF, 0xF, false));
  v = max(v, (unsigned)__builtin_amdgcn_update_dpp(0u, v, 0x118, 0xF, 0xF, false));
  v = max(v, (unsigned)__builtin_amdgcn_update_dpp(0u, v, 0x142, 0xA, 0xF, false));
  v = max(v, (unsigned)__builtin_amdgcn_update_dpp(0u, v, 0x143, 0xC, 0xF, false));
  return v;
}
DI unsigned row_max_u32(unsigned v) {
  v = max(v, (unsigned)__builtin_amdgcn_update_dpp(0u, v, 0x111, 0xF, 0xF, false));
  v = max(v, (unsigned)__builtin_amdgcn_update_dpp(0u, v, 0x112, 0xF, 0xF, false));
  v = max(v, (unsigned)__builtin_amdgcn_update_dpp(0u, v, 0x114, 0xF, 0xF, false));
  v = max(v, (unsigned)__builtin_amdgcn_update_dpp(0u, v, 0x118, 0xF, 0xF, false));
  return v;
}
DI unsigned absbits(float f) { return __float_as_uint(f) & 0x7FFFFFFFu; }
DI void lds_max(unsigned addr, unsigned v) {
  __hip_atomic_fetch_max((LDS_AS unsigned*)(size_t)addr, v, __ATOMIC_RELAXED, __HIP_MEMORY_SCOPE_WORKGROUP);
}
DI int gstart(int g) { return (g * NN + (NG - 1)) / NG; }

constexpr int PB_T1 = 196;
constexpr int PB_T2 = PB_T1 + NCH;
constexpr int PB_WB = PB_T2 + 8;
constexpr int PB_W2 = PB_WB + 8;
constexpr int PB_TBL = PB_W2 + 8;
constexpr int PREP_LDS = 65536 + 64;

__global__ __launch_bounds__(512) void prep_kernel(const unsigned short* __restrict__ x, const uint32_t* __restrict__ ea,
                                                   const int* __restrict__ src, const int* __restrict__ dst,
                                                   const unsigned short* __restrict__ w1, const unsigned short* __restrict__ w2,
                                                   char* __restrict__ ws) {
  __shared__ __attribute__((aligned(16))) char lds[PREP_LDS];
  const unsigned lb = (unsigned)(size_t)lds;
  const int tid = threadIdx.x, lane = tid & 63, wv = tid >> 6;
  const int blk = blockIdx.x;
  PSTAMP(0);
  if (blk < PB_T1) {
    const int n0 = (blk >> 1) * 128, half = blk & 1;
    const unsigned XO = lb, WO = lb + 32768;
    const __amdgpu_buffer_rsrc_t srdPw = __builtin_amdgcn_make_buffer_rsrc((void*)(ws + WS_P), (short)0, NN * 512, 0x00020000);
    u32x4 xv[4], wv4[4];
#pragma unroll
    for (int k = 0; k < 4; ++k) {
      const int q = tid + 512 * k, row = q >> 4, ch = q & 15;
      const int n = min(n0 + row, NN - 1);
      xv[k] = *(const u32x4*)(x + (size_t)n * 128 + ch * 8);
      wv4[k] = *(const u32x4*)(w1 + (size_t)row * 256 + half * 128 + ch * 8);
    }
#pragma unroll
    for (int k = 0; k < 4; ++k) {
      const int q = tid + 512 * k, row = q >> 4, ch = q & 15;
      lds_st<u32x4>(XO + row * 256 + ((ch ^ (row & 15)) << 4), xv[k]);
      lds_st<u32x4>(WO + row * 256 + ((ch ^ (((row & 3) << 2) | ((row >> 2) & 3))) << 4), wv4[k]);
    }
    if (tid == 0) lds_st<unsigned>(lb + 65536, 0u);
    __syncthreads();
    PSTAMP(1);
    const int nt = wv & 3, cp = wv >> 2;
    const int r = lane & 31, h = lane >> 5, bk = (lane >> 4) & 1, q4 = (lane & 15) >> 2, p4 = lane & 3;
    f32x16 acc[2];
#pragma unroll
    for (int i = 0; i < 16; ++i) { acc[0][i] = 0.f; acc[1][i] = 0.f; }
    const int xrow = 32 * nt + r;
    const unsigned xb = XO + xrow * 256 + ((h ^ (xrow & 15)) << 4);
    const int chw = 8 * cp + 2 * bk + (p4 >> 1), mA = (q4 << 2) | (2 * h), mB = mA | 1;
    const unsigned wa0 = WO + (8 * h + q4) * 256 + ((chw ^ mA) << 4) + 8 * (p4 & 1);
    const unsigned wa1 = WO + (8 * h + q4 + 4) * 256 + ((chw ^ mB) << 4) + 8 * (p4 & 1);
#pragma unroll
    for (int ks = 0; ks < 8; ++ks) {
      const u32x4 bfr = lds_ld<u32x4>(xb ^ (unsigned)(ks << 5));
#pragma unroll
      for (int ct = 0; ct < 2; ++ct) {
        const s16x4 lo = __builtin_amdgcn_ds_read_tr16_b64_v4i16((LDS_AS s16x4*)(size_t)((wa0 ^ (unsigned)(ct << 6)) + ks * 4096));
        const s16x4 hi = __builtin_amdgcn_ds_read_tr16_b64_v4i16((LDS_AS s16x4*)(size_t)((wa1 ^ (unsigned)(ct << 6)) + ks * 4096));
        const bf16x8 afr = __builtin_shufflevector(lo, hi, 0, 1, 2, 3, 4, 5, 6, 7);
        acc[ct] = mfma(__builtin_bit_cast(u32x4, afr), bfr, acc[ct]);
      }
    }
    __syncthreads();
    PSTAMP(2);
#pragma unroll
    for (int ct = 0; ct < 2; ++ct) {
      const float sc = ct ? LOG2E : LSCALE;
#pragma unroll
      for (int g = 0; g < 4; ++g) {
        u32x2 pk;
        pk[0] = pack_bf16(acc[ct][4 * g] * sc, acc[ct][4 * g + 1] * sc);
        pk[1] = pack_bf16(acc[ct][4 * g + 2] * sc, acc[ct][4 * g + 3] * sc);
        lds_st<u32x2>(lb + xrow * 272 + (64 * cp + 32 * ct + 8 * g + 4 * h) * 2, pk);
      }
    }
    __syncthreads();
#pragma unroll
    for (int k = 0; k < 4; ++k) {
      const int q = tid + 512 * k, row = q >> 4, ch = q & 15;
      __builtin_amdgcn_raw_buffer_store_b128(lds_ld<u32x4>(lb + row * 272 + (ch << 4)), srdPw, (n0 + row) * 512 + half * 256 + ch * 16, 0, STORE_AUX);
    }
    {
      int mi = 0;
      unsigned mu = 0;
#pragma unroll
      for (int i = 0; i < 16; ++i) {
        const unsigned a = __float_as_uint(acc[0][i]), b = __float_as_uint(acc[1][i]);
        mi = max(mi, max((int)a, (int)b));
        mu = max(mu, max(a, b));
      }
      const unsigned m = wave_max_u32(max((unsigned)mi, mu & 0x7FFFFFFFu));
      if (lane == 63) lds_max(lb + 65536, m);
    }
    __syncthreads();
    if (tid == 0) ((unsigned*)(ws + WS_MX))[blk] = lds_ld<unsigned>(lb + 65536);
  } else if (blk < PB_T2) {
    const int bi = blk - PB_T1;
    const int b = ((blk & 7) << 5) + (bi >> 3);
    const unsigned HO = lb, SO = lb + 2048, RO = lb + 4096;
    const __amdgpu_buffer_rsrc_t srdRw = __builtin_amdgcn_make_buffer_rsrc((void*)(ws + WS_REC + (size_t)b * CHP * 12), (short)0, CHP * 12, 0x00020000);
    lds_st<unsigned>(HO + tid * 4, 0u);
    if (tid == 0) lds_st<unsigned>(SO + 128, 0u);
    int dv[4], sv[4];
    u32x2 ev[4];
#pragma unroll
    for (int k = 0; k < 4; ++k) {
      const int i = tid + 512 * k, e = b * CH + i;
      const bool ok = i < CH && e < NE;
      const int ec = ok ? e : 0;
      dv[k] = dst[ec];
      sv[k] = src[ec];
      ev[k] = *(const u32x2*)(ea + (size_t)ec * 2);
      if (!ok) dv[k] = -1;
    }
    __syncthreads();
    PSTAMP(1);
    int gk[4];
    unsigned rk[4];
#pragma unroll
    for (int k = 0; k < 4; ++k) {
      gk[k] = (int)((float)dv[k] * ((float)NG / (float)NN) + 1e-4f);
      rk[k] = __hip_atomic_fetch_add((LDS_AS unsigned*)(size_t)(dv[k] >= 0 ? HO + gk[k] * 4 : SO + 64), 1u, __ATOMIC_RELAXED, __HIP_MEMORY_SCOPE_WORKGROUP);
    }
    __syncthreads();
    PSTAMP(2);
    const unsigned c = lds_ld<unsigned>(HO + tid * 4);
    const unsigned inc = wave_incl_scan(c);
    if (lane == 63) lds_st<unsigned>(SO + wv * 4, inc);
    __syncthreads();
    const u32x4 t0 = lds_ld<u32x4>(SO), t1 = lds_ld<u32x4>(SO + 16);
    unsigned pre = 0;
    pre += wv > 0 ? t0[0] : 0u; pre += wv > 1 ? t0[1] : 0u; pre += wv > 2 ? t0[2] : 0u; pre += wv > 3 ? t0[3] : 0u;
    pre += wv > 4 ? t1[0] : 0u; pre += wv > 5 ? t1[1] : 0u; pre += wv > 6 ? t1[2] : 0u;
    const unsigned start = pre + inc - c;
    ((uint32_t*)(ws + WS_TAB))[tid * NCH + b] = start | (c << 16);
    lds_st<unsigned>(HO + tid * 4, start);
    __syncthreads();
#pragma unroll
    for (int k = 0; k < 4; ++k) {
      if (dv[k] >= 0) {
        const unsigned st = lds_ld<unsigned>(HO + gk[k] * 4);
        const unsigned ra = RO + (st + rk[k]) * 12;
        lds_st<unsigned>(ra, ((unsigned)sv[k] << 9) | (((unsigned)(dv[k] * NG - gk[k] * NN) >> 5) & 0x1F0u));
        lds_st<unsigned>(ra + 4, ev[k][0]);
        lds_st<unsigned>(ra + 8, ev[k][1]);
      }
    }
    __syncthreads();
#pragma unroll
    for (int k = 0; k < 3; ++k) {
      const int i = tid + 512 * k;
      if (i < CHP * 12 / 16) __builtin_amdgcn_raw_buffer_store_b128(lds_ld<u32x4>(RO + i * 16), srdRw, i * 16, 0, STORE_AUX);
    }
    {
      u16x2 pm = {0, 0};
#pragma unroll
      for (int k = 0; k < 4; ++k) {
        pm = __builtin_elementwise_max(pm, __builtin_bit_cast(u16x2, ev[k][0] & 0x7FFF7FFFu));
        pm = __builtin_elementwise_max(pm, __builtin_bit_cast(u16x2, ev[k][1] & 0x7FFF7FFFu));
      }
      const unsigned m = wave_max_u32((unsigned)max(pm[0], pm[1]) << 16);
      if (lane == 63) lds_max(SO + 128, m);
    }
    __syncthreads();
    if (tid == 0) ((unsigned*)(ws + WS_MX))[256 + b] = lds_ld<unsigned>(SO + 128);
  } else if (blk < PB_WB) {
    const int q = (blk - PB_T2) * 512 + tid;
    const int l = q & 63, ks = (q >> 6) & 7, ct = q >> 9;
    const int col = 32 * ct + (l & 31), k0 = 128 + 16 * ks + 8 * (l >> 5);
    unsigned short v[8];
#pragma unroll
    for (int j = 0; j < 8; ++j) v[j] = w1[(size_t)(k0 + j) * 256 + col];
    u32x4 o;
#pragma unroll
    for (int j = 0; j < 4; ++j) o[j] = v[2 * j] | ((unsigned)v[2 * j + 1] << 16);
    *(u32x4*)(ws + WS_WB + (size_t)q * 16) = o;
  } else if (blk >= PB_W2) {
    const unsigned c0 = (unsigned)((blk - PB_W2) * 512 + tid) * 4;
    u32x2 tp, tn;
#pragma unroll
    for (int j = 0; j < 2; ++j) {
      const unsigned b0 = ((c0 + 2 * j) << 16) | 0x8000u;
      const float za = __uint_as_float(b0) * 64.0f, zb = __uint_as_float(b0 + 0x10000u) * 64.0f;
      const float ea_ = __builtin_amdgcn_exp2f(-LOG2E * za), eb_ = __builtin_amdgcn_exp2f(-LOG2E * zb);
      const float ra = __builtin_amdgcn_rcpf(1.0f + ea_), rb = __builtin_amdgcn_rcpf(1.0f + eb_);
      tp[j] = pack_bf16(za * ra, zb * rb);
      tn[j] = pack_bf16(-(za * ea_) * ra, -(zb * eb_) * rb);
    }
    *(u32x2*)(ws + WS_TBL + c0 * 2) = tp;
    *(u32x2*)(ws + WS_TBL + 32768 + c0 * 2) = tn;
  } else {
    const int q = (blk - PB_WB) * 512 + tid;
    const int l = q & 63, ot = (q >> 6) & 3, ks = q >> 8;
    const int col = 32 * ot + (l & 31), h = l >> 5;
    unsigned short v[8];
#pragma unroll
    for (int j = 0; j < 8; ++j) v[j] = w2[(size_t)(32 * (ks >> 1) + crow(8 * (ks & 1) + j, h)) * 128 + col];
    u32x4 o;
#pragma unroll
    for (int j = 0; j < 4; ++j) o[j] = v[2 * j] | ((unsigned)v[2 * j + 1] << 16);
    *(u32x4*)(ws + WS_W2 + (size_t)q * 16) = o;
  }
  PSTAMP(3);
}

constexpr unsigned L_TP = 0;
constexpr unsigned L_WORD = 32768;
constexpr unsigned WORD_TEAM = 36 * 128;
constexpr unsigned L_MASK = L_WORD + 2 * WORD_TEAM;
constexpr unsigned MASK_TEAM = 34 * 128;
constexpr unsigned L_CNT = L_MASK + 2 * MASK_TEAM;
constexpr unsigned L_TOT = L_CNT + 256;
constexpr unsigned L_MAX = L_TOT + 16;
constexpr unsigned L_PROG = L_TOT + 32;
constexpr unsigned L_BKT = L_TOT + 64;
constexpr unsigned L_ZEND = L_TOT + 128;
constexpr unsigned L_ALUT = (L_ZEND + 511) / 512 * 512;
constexpr unsigned L_BLUT = L_ALUT + 2048;
constexpr unsigned L_EA = L_BLUT + 4096;
constexpr unsigned L_AEND = L_EA + 4096;
constexpr unsigned L_TN = 65536;
constexpr unsigned L_STG = 98304;
constexpr unsigned L_END = 163840;
static_assert(L_AEND <= 65536 && (L_ZEND - L_MASK) % 16 == 0 && L_ALUT % 512 == 0, "LDS region A");
constexpr unsigned PADWORD = 0x7FFFFE00u | (31u << 4);

__global__ __launch_bounds__(512, 2) void main_kernel(const unsigned short* __restrict__ x, const unsigned short* __restrict__ w1,
                                                      const unsigned short* __restrict__ b1, const unsigned short* __restrict__ b2,
                                                      char* __restrict__ ws, unsigned short* __restrict__ out) {
  __shared__ __attribute__((aligned(16))) char lds[L_END];
  asm volatile("" ::"s"((unsigned)(size_t)lds));
  const int tid = threadIdx.x, lane = tid & 63;
  const int wv = __builtin_amdgcn_readfirstlane(tid >> 6);
  const int team = wv >> 2, w = wv & 3, tt = tid & 255;
  const int wg = (blockIdx.x & 7) * 32 + (blockIdx.x >> 3);
  const int g = 2 * wg + team;
  const int n0 = gstart(g), G = gstart(g + 1) - n0;
  const int r = lane & 31, h = lane >> 5;

  const __amdgpu_buffer_rsrc_t srdP = __builtin_amdgcn_make_buffer_rsrc((void*)(ws + WS_P - 3072), (short)0, NN * 512 + 3072, 0x00020000);
  STAMP(0);
  const uint32_t tabv = ((const uint32_t*)(ws + WS_TAB))[g * NCH + tt];
  u32x4 xs[2];
#pragma unroll
  for (int k = 0; k < 2; ++k) {
    const int q = tt + 256 * k, row = q >> 4, ch = q & 15;
    const int n = min(n0 + row, NN - 1);
    xs[k] = *(const u32x4*)(x + (size_t)n * 128 + ch * 8);
  }
  unsigned mxv = ((const unsigned*)(ws + WS_MX))[tid];
  const unsigned short cb0 = b1[32 * wv + r];
  const unsigned short cb1 = w1[(size_t)(256 + 2 * h) * 256 + 32 * wv + r];
  const unsigned short cb2 = w1[(size_t)(257 + 2 * h) * 256 + 32 * wv + r];

  STAMP(1);
  {
    const u32x4 pad = {PADWORD, PADWORD, PADWORD, PADWORD}, zero = {0, 0, 0, 0};
    lds_st<u32x4>(L_WORD + tid * 16, pad);
    if (tid < (int)(2 * WORD_TEAM / 16 - 512)) lds_st<u32x4>(L_WORD + (512 + tid) * 16, pad);
    lds_st<u32x4>(L_MASK + tid * 16, zero);
    if (tid < (int)((L_ZEND - L_MASK) / 16 - 512)) lds_st<u32x4>(L_MASK + (512 + tid) * 16, zero);
  }
  if (tid < 128) {
    const int u = tid & 31, hh = (tid >> 5) & 1, s = tid >> 6;
    const int rho = u < 26 ? u : u + 2;
    const int i = (rho & 3) + 4 * (rho >> 3), j = i & 7;
    const bool on = u < 28 && ((rho >> 2) & 1) == hh && (i >> 3) == s;
    const unsigned one = on ? (0x3F80u << (16 * (j & 1))) : 0u;
    u32x4 v;
#pragma unroll
    for (int q = 0; q < 4; ++q) v[q] = (j >> 1) == q ? one : 0u;
    lds_st<u32x4>(L_ALUT + tid * 16, v);
  } else if (tid < 384) {
    const int bb = tid - 128;
    u32x4 v;
#pragma unroll
    for (int j = 0; j < 4; ++j) v[j] = ((bb >> (2 * j)) & 1 ? 0x3F80u : 0u) | ((bb >> (2 * j + 1)) & 1 ? 0x3F800000u : 0u);
    lds_st<u32x4>(L_BLUT + bb * 16, v);
  }
  STAMP(2);
  u32x4 wb[8];
#pragma unroll
  for (int k = 0; k < 8; ++k) wb[k] = *(const u32x4*)(ws + WS_WB + (size_t)(wv * 8 + k) * 1024 + lane * 16);

  const unsigned wbase = L_WORD + team * WORD_TEAM;
  const unsigned stg = L_STG + wv * 8192;
  const unsigned lc0 = (unsigned)(w * 128 + (((lane & 7) ^ ((lane >> 4) & 3)) << 4));
  const unsigned lc1 = lc0 ^ 64u;
  const unsigned w4a = wbase + (lane >> 3) * 16;
  const unsigned eav = (unsigned)((2 * r + h) * 4);
  STAMP(3);

  auto xaddr = [&](int tm, int row, int ch) { return L_STG + (unsigned)(2 * tm + (row >> 4)) * 8192 + (unsigned)(row & 15) * 256 + (unsigned)((ch ^ (row & 15)) << 4); };
  auto qaddr = [&](int tm, int ct, int s) { const int i = (tm * 8 + ct) * 2 + s; return L_STG + (unsigned)(i >> 2) * 8192 + 4096 + (unsigned)(i & 3) * 1024 + lane * 16; };
  const unsigned IDX = L_STG + (4 + team) * 8192;
#pragma unroll
  for (int k = 0; k < 2; ++k) {
    const int q = tt + 256 * k, row = q >> 4, ch = q & 15;
    u32x4 v = xs[k];
    if (row >= G) v = u32x4{0, 0, 0, 0};
    lds_st<u32x4>(xaddr(team, row, ch), v);
  }
  STAMP(4);
  __syncthreads();

  STAMP(5);
  const unsigned segs = tabv & 0xFFFFu, segc = tabv >> 16;
  const unsigned inc = wave_incl_scan(segc);
  unsigned wbase0 = 0;
  if (lane == 63) wbase0 = __hip_atomic_fetch_add((LDS_AS unsigned*)(size_t)(L_TOT + team * 4), inc, __ATOMIC_RELAXED, __HIP_MEMORY_SCOPE_WORKGROUP);
  const unsigned pos0 = __builtin_amdgcn_readlane(wbase0, 63) + inc - segc;
  const unsigned rec0 = (unsigned)tt * CHP + segs;
  char* eap = ws + WS_EA + (size_t)g * CAP * 8;
  {
    const unsigned kend = pos0 < (unsigned)CAP ? min(segc, (unsigned)CAP - pos0) : 0u;
    for (unsigned k = 0; k < kend; ++k) lds_st<unsigned>(IDX + (pos0 + k) * 4, rec0 + k);
  }
  STAMP(6);
  __syncthreads();

  unsigned total, tot0;
  int nrounds;
  {
    const unsigned t0 = lds_ld<unsigned>(L_TOT), t1 = lds_ld<unsigned>(L_TOT + 4);
    tot0 = __builtin_amdgcn_readfirstlane(t0);
    total = __builtin_amdgcn_readfirstlane(team ? t1 : t0);
    nrounds = __builtin_amdgcn_readfirstlane((int)((max(t0, t1) + CAP - 1) / CAP));
  }
  STAMP(7);
  u32x3 rec[4];
  auto fetch_records = [&](unsigned rem) {
#pragma unroll
    for (int k = 0; k < 4; ++k) {
      const unsigned p = tt + 256 * k;
      const unsigned ri = lds_ld<unsigned>(IDX + p * 4);
      rec[k] = *(const u32x3_a4*)(ws + WS_REC + (size_t)(p < rem ? ri : 0u) * 12);
    }
  };
  fetch_records(min(total, (unsigned)CAP));
  STAMP(8);
  {
    const int u = r < 26 ? r : (r == 28 || r == 29) ? r - 2 : 31;
    const float sc = (wv & 1) ? LOG2E : LSCALE;
    const float c0s = bf2f(cb0) * sc, cin1 = bf2f(cb1), cin2 = bf2f(cb2);
    const unsigned xa = xaddr(0, u, h);
    f32x16 acc[2];
    u32x4 af[2][8];
#pragma unroll
    for (int tm = 0; tm < 2; ++tm) {
#pragma unroll
      for (int ks = 0; ks < 8; ++ks) af[tm][ks] = lds_ld<u32x4>((xa ^ (unsigned)(ks << 5)) + tm * 16384);
#pragma unroll
      for (int i = 0; i < 16; ++i) acc[tm][i] = 0.f;
    }
    __builtin_amdgcn_sched_barrier(0);
#pragma unroll
    for (int ks = 0; ks < 8; ++ks) {
      acc[0] = mfma(af[0][ks], wb[ks], acc[0]);
      acc[1] = mfma(af[1][ks], wb[ks], acc[1]);
    }
    int mi = 0;
    unsigned mu = 0;
    const unsigned wrow = pack_bf16(cin1 * sc, cin2 * sc);
#pragma unroll
    for (int tm = 0; tm < 2; ++tm) {
      float q[14];
#pragma unroll
      for (int i = 0; i < 14; ++i) q[i] = __builtin_fmaf(acc[tm][i], sc, c0s);
#pragma unroll
      for (int i = 0; i < 14; i += 2) {
        const unsigned a = __float_as_uint(q[i]), b = __float_as_uint(q[i + 1]);
        mi = max(mi, max((int)a, (int)b));
        mu = max(mu, max(a, b));
      }
      lds_st<u32x4>(qaddr(tm, wv, 0), u32x4{pack_bf16(q[0], q[1]), pack_bf16(q[2], q[3]), pack_bf16(q[4], q[5]), pack_bf16(q[6], q[7])});
      lds_st<u32x4>(qaddr(tm, wv, 1), u32x4{pack_bf16(q[8], q[9]), pack_bf16(q[10], q[11]), pack_bf16(q[12], q[13]), wrow});
    }
    const unsigned mq = row_max_u32(__float_as_uint(__uint_as_float(max((unsigned)mi, mu & 0x7FFFFFFFu)) * (1.0f / sc)));
    const unsigned mw = row_max_u32(max(absbits(cin1), absbits(cin2)));
    const unsigned mx = row_max_u32(tid < PB_T1 || tid >= 256 ? mxv : 0u);
    if ((lane & 15) == 15)
      asm volatile("ds_max_u32 %0, %1\n\tds_max_u32 %2, %3 offset:8\n\tds_max_u32 %2, %4 offset:12" ::"v"(L_MAX + (team ? 4u : 0u)), "v"(mx), "v"(L_MAX), "v"(mq), "v"(mw) : "memory");
  }
  {
    const __amdgpu_buffer_rsrc_t srdT = __builtin_amdgcn_make_buffer_rsrc((void*)(ws + WS_TBL), (short)0, 65536, 0x00020000);
    const unsigned vt = (unsigned)(wv * 8192 + lane * 16), mt = (wv < 4 ? L_TP : L_TN - 32768) + wv * 8192;
    asm volatile(
        "s_mov_b32 m0, %1\n\ts_nop 0\n\tbuffer_load_dwordx4 %0, %3, 0 offen lds\n\t"
        "buffer_load_dwordx4 %0, %3, 0 offen offset:1024 lds\n\t"
        "buffer_load_dwordx4 %0, %3, 0 offen offset:2048 lds\n\t"
        "buffer_load_dwordx4 %0, %3, 0 offen offset:3072 lds\n\t"
        "s_mov_b32 m0, %2\n\ts_nop 0\n\tbuffer_load_dwordx4 %0, %3, %4 offen lds\n\t"
        "buffer_load_dwordx4 %0, %3, %4 offen offset:1024 lds\n\t"
        "buffer_load_dwordx4 %0, %3, %4 offen offset:2048 lds\n\t"
        "buffer_load_dwordx4 %0, %3, %4 offen offset:3072 lds"
        ::"v"(vt), "s"(mt), "s"(mt + 4096), "s"(srdT), "s"(4096)
        : "memory");
  }
  STAMP(9);
  f32x16 S[2];
#pragma unroll
  for (int i = 0; i < 16; ++i) { S[0][i] = 0.f; S[1][i] = 0.f; }
  auto place_at = [&](unsigned p, const u32x3& rc) {
    const unsigned tile = p >> 5, rr = p & 31;
    lds_st<unsigned>(wbase + tile * 128 + ((rr & 7) * 4 + (rr >> 3)) * 4, rc[0]);
    const unsigned u = (rc[0] >> 4) & 31;
    const unsigned bit = 16 * (rr >> 4) + 8 * ((rr >> 2) & 1) + 4 * ((rr >> 3) & 1) + (rr & 3);
    __hip_atomic_fetch_or((LDS_AS unsigned*)(size_t)(L_MASK + team * MASK_TEAM + tile * 128 + u * 4), 1u << bit, __ATOMIC_RELAXED, __HIP_MEMORY_SCOPE_WORKGROUP);
    __hip_atomic_fetch_add((LDS_AS unsigned*)(size_t)(L_CNT + team * 128 + u * 4), 1u, __ATOMIC_RELAXED, __HIP_MEMORY_SCOPE_WORKGROUP);
    *(u32x2*)(eap + (size_t)p * 8) = u32x2{rc[1], rc[2]};
  };
  const unsigned rem0 = min(total, (unsigned)CAP);
  {
    unsigned bk[4], rk[4];
#pragma unroll
    for (int k = 0; k < 4; ++k) {
      bk[k] = rec[k][0] >> 20;
      rk[k] = 0;
      if (tt + 256u * k < rem0) rk[k] = __hip_atomic_fetch_add((LDS_AS unsigned*)(size_t)(L_BKT + team * 32 + bk[k] * 4), 1u, __ATOMIC_RELAXED, __HIP_MEMORY_SCOPE_WORKGROUP);
    }
    asm volatile("s_waitcnt lgkmcnt(0)" ::: "memory");
    __builtin_amdgcn_s_barrier();
    const unsigned cnt8 = lds_ld<unsigned>(L_BKT + team * 32 + (lane & 7) * 4);
    const unsigned pre8 = wave_incl_scan(lane < 8 ? cnt8 : 0u) - cnt8;
#pragma unroll
    for (int k = 0; k < 4; ++k) {
      const unsigned base = __builtin_amdgcn_ds_bpermute(bk[k] * 4, pre8);
      if (tt + 256u * k < rem0) place_at(base + rk[k], rec[k]);
    }
  }
  STAMP(10);
  asm volatile("s_waitcnt lgkmcnt(0)" ::: "memory");
  __builtin_amdgcn_s_barrier();
  STAMP(11);

  auto dmaP = [&](int B, u32x4 wd) {
    const unsigned v0 = (wd[0] & 0xFFFFFE00u) | lc0, v1 = (wd[1] & 0xFFFFFE00u) | lc1;
    const unsigned v2 = (wd[2] & 0xFFFFFE00u) | lc0, v3 = (wd[3] & 0xFFFFFE00u) | lc1;
    const unsigned m0p = stg + B * 4096;
    asm volatile(
        "s_mov_b32 m0, %4\n\ts_nop 0\n\tbuffer_load_dwordx4 %0, %5, %6 offen lds\n\t"
        "buffer_load_dwordx4 %1, %5, %7 offen offset:1024 lds\n\t"
        "buffer_load_dwordx4 %2, %5, %8 offen offset:2048 lds\n\t"
        "buffer_load_dwordx4 %3, %5, 0 offen offset:3072 lds"
        ::"v"(v0), "v"(v1), "v"(v2), "v"(v3), "s"(m0p), "s"(srdP), "s"(3072), "s"(2048), "s"(1024)
        : "memory");
  };
  auto dmaE = [&](int T, int B, __amdgpu_buffer_rsrc_t srdE) {
    const unsigned ve = eav + (unsigned)T * 256u;
    const unsigned m0e = L_EA + wv * 512 + B * 256;
    asm volatile("s_mov_b32 m0, %1\n\ts_nop 0\n\tbuffer_load_dword %0, %2, 0 offen lds" ::"v"(ve), "s"(m0e), "s"(srdE) : "memory");
  };
  const __amdgpu_buffer_rsrc_t srdE0 = __builtin_amdgcn_make_buffer_rsrc((void*)eap, (short)0, (int)(rem0 * 8), 0x00020000);
  dmaP(0, lds_ld<u32x4>(w4a));

  u32x4 qf[2][2];
#pragma unroll
  for (int t = 0; t < 2; ++t)
#pragma unroll
    for (int s = 0; s < 2; ++s) qf[t][s] = lds_ld<u32x4>(qaddr(team, 2 * w + t, s));
  bool fast;
  {
    const u32x4 mx = lds_ld<u32x4>(L_MAX);
    const float bound = __uint_as_float(mx[0]) + __uint_as_float(mx[2]) + 4.0f * __uint_as_float(mx[1]) * __uint_as_float(mx[3]);
    fast = __builtin_amdgcn_readfirstlane(bound < 120.0f ? 1 : 0) != 0 && nrounds <= 1;
  }
  asm volatile("" ::"v"(qf[0][0]), "v"(qf[0][1]), "v"(qf[1][0]), "v"(qf[1][1]));
  STAMP(19);
  u32x4 idn[2];
#pragma unroll
  for (int s = 0; s < 2; ++s) {
    const unsigned d = (unsigned)(r - 16 * s - 8 * h);
    idn[s] = lds_ld<u32x4>(L_BLUT + (d < 8u ? (16u << d) : 0u));
  }
  const unsigned pb = (unsigned)(r * 128 + ((h ^ ((r >> 1) & 7)) << 4));
  unsigned pa0 = stg + pb, pa1 = stg + (pb ^ 32u), pa2 = stg + (pb ^ 64u), pa3 = stg + (pb ^ 96u);
  unsigned w1a = wbase + ((r & 7) * 4 + (r >> 3)) * 4;
  unsigned mka = L_MASK + team * MASK_TEAM + r * 4 + h;
  unsigned eal = L_EA + wv * 512 + lane * 4;
  unsigned alh = L_ALUT + h * 512;
  asm volatile("" : "+v"(idn[0]), "+v"(idn[1]), "+v"(pa0), "+v"(pa1), "+v"(pa2), "+v"(pa3), "+v"(w1a), "+v"(mka), "+v"(eal), "+v"(alh));
  {
    const u32x4 wd_1 = lds_ld<u32x4>(w4a + 128);
    STAMP(20);
    asm volatile("s_waitcnt vmcnt(4) lgkmcnt(0)" ::: "memory");
    STAMP(21);
    __builtin_amdgcn_s_barrier();
    dmaE(0, 0, srdE0);
    dmaP(1, wd_1);
    dmaE(1, 1, srdE0);
  }
  STAMP(12);
  auto load_ops = [&](unsigned wd, int B, u32x4 (&pp)[4], u32x4 (&aa)[2]) {
    const unsigned ad = (wd & 0x1F0u) | alh;
    aa[0] = lds_ld<u32x4>(ad);
    const u32x3 a1 = lds_ld<u32x3>(ad + 1024);
    aa[1][0] = a1[0]; aa[1][1] = a1[1]; aa[1][2] = a1[2];
    aa[1][3] = lds_ld<unsigned>(eal + B * 256);
    pp[0] = lds_ld<u32x4>(pa0 + B * 4096);
    pp[1] = lds_ld<u32x4>(pa1 + B * 4096);
    pp[2] = lds_ld<u32x4>(pa2 + B * 4096);
    pp[3] = lds_ld<u32x4>(pa3 + B * 4096);
  };
#define SB() __builtin_amdgcn_sched_barrier(0)
#define KEEP_OPS() asm volatile("" ::"v"(pp[0]), "v"(pp[1]), "v"(pp[2]), "v"(pp[3]), "v"(aa[1]))
  f32x16 zz;
#pragma unroll
  for (int i = 0; i < 16; ++i) zz[i] = 0.f;

  u32x4 w2f[16];
  u32x2 b2v[4];
  bool w2_loaded = false;
  auto load_w2 = [&]() {
#pragma unroll
    for (int ks = 0; ks < 16; ++ks) w2f[ks] = *(const u32x4*)(ws + WS_W2 + (size_t)(ks * 4 + w) * 1024 + lane * 16);
#pragma unroll
    for (int gq = 0; gq < 4; ++gq) b2v[gq] = *(const u32x2*)(b2 + 32 * w + 8 * gq + 4 * h);
    w2_loaded = true;
  };
  if (fast) {
    const int ntiles = (int)((rem0 + 31) >> 5);
    if (ntiles > 0) {
      u32x4 pp[4], aa[2];
      asm volatile("s_waitcnt vmcnt(5)" ::: "memory");
      STAMP(14);
      load_ops(lds_ld<unsigned>(w1a), 0, pp, aa);
      {
        const u32x4 wd = lds_ld<u32x4>(w4a + 256);
        KEEP_OPS();
        dmaP(0, wd);
        dmaE(2, 0, srdE0);
      }
      const float n0f = (float)((min(tot0, (unsigned)CAP) + 31) >> 5), n1f = (float)ntiles;
      const int tflip = __builtin_amdgcn_readfirstlane(team ? (int)((n1f - PRIO_RHO * n0f) * (1.0f / (1.0f - PRIO_RHO * PRIO_RHO))) : -1);
      asm volatile("s_cmp_lt_i32 %0, 1\n\ts_cbranch_scc1 1f\n\ts_setprio 1\n1:" ::"s"(tflip) : "scc");
      STAMP(15);
      CSTAMP(30);
      u32x4 bp[2], bn[2], h0[2], h1[2], wd4;
      f32x16 z0, z1, z1p;
      unsigned lk[16], mb0, mb1, wd1;
      f32x2_t E[8], R[8], M[8];
#pragma unroll
      for (int i = 0; i < 16; ++i) { z1p[i] = 0.f; lk[i] = 0; }
      bp[0] = u32x4{0, 0, 0, 0};
      bp[1] = bp[0];
#define VE(j) E[j] = f32x2_t{__builtin_amdgcn_exp2f(-z1p[2 * (j)]), __builtin_amdgcn_exp2f(-z1p[2 * (j) + 1])}
#define VA(j) E[j] = E[j] + 1.0f
#define VR(j) R[j] = f32x2_t{__builtin_amdgcn_rcpf(E[j][0]), __builtin_amdgcn_rcpf(E[j][1])}
#define VM(j) M[j] = f32x2_t{z1p[2 * (j)], z1p[2 * (j) + 1]} * R[j]
#define VC(j) h1[(j) >> 2][(j) & 3] = pack_bf16(M[j][0], M[j][1])
#define MG(j) h0[(j) >> 2][(j) & 3] = lk[2 * (j)] | (lk[2 * (j) + 1] << 16)
#define LL(i) lk[i] = lds_ld<unsigned short>((__float_as_uint(z0[i]) >> 16) << 1)
      auto partA = [&](int T) {
        z0 = mfma(aa[0], qf[0][0], zz); VE(0); MG(0); SB();
        z0 = mfma(aa[1], qf[0][1], z0); VE(1); VA(0); MG(1); SB();
        z0 = mfma(pp[0], idn[0], z0); VE(2); VA(1); VR(0); MG(2);
        mb0 = lds_ld<unsigned char>(mka + T * 128); mb1 = lds_ld<unsigned char>(mka + T * 128 + 2);
        wd1 = lds_ld<unsigned>(w1a + (T + 1) * 128); wd4 = lds_ld<u32x4>(w4a + (T + 3) * 128);
        SB();
        z0 = mfma(pp[1], idn[1], z0); VE(3); VA(2); VR(1); VM(0); MG(3); SB();
        z1 = mfma(aa[0], qf[1][0], zz); VE(4); VA(3); VR(2); VM(1); VC(0); MG(4); SB();
      };
      auto partB = [&](int T, auto BC) {
        constexpr int B = decltype(BC)::value;
        u32x4 pn[4], an[2];
        asm volatile("s_waitcnt vmcnt(5)" ::: "memory");
        load_ops(wd1, B, pn, an);
        z1 = mfma(aa[1], qf[1][1], z1); VE(5); VA(4); VR(3); VM(2); VC(1); MG(5); LL(0); LL(1); SB();
        z1 = mfma(pp[2], idn[0], z1); VE(6); VA(5); VR(4); VM(3); VC(2); MG(6); LL(2); LL(3); SB();
        z1 = mfma(pp[3], idn[1], z1); VE(7); VA(6); VR(5); VM(4); VC(3); MG(7); LL(4); LL(5);
        asm volatile("" ::"v"(pn[0]), "v"(pn[1]), "v"(pn[2]), "v"(pn[3]), "v"(an[1]));
        dmaP(B, wd4);
        dmaE(T + 3, B, srdE0);
        SB();
        S[0] = mfma(h0[0], bp[0], S[0]); VA(7); VR(6); VM(5); VC(4); LL(6); LL(7); LL(8); SB();
        S[0] = mfma(h0[1], bp[1], S[0]); VR(7); VM(6); VC(5); LL(9); LL(10); LL(11);
        bn[0] = lds_ld<u32x4>(L_BLUT + mb0 * 16); bn[1] = lds_ld<u32x4>(L_BLUT + mb1 * 16);
        SB();
        S[1] = mfma(h1[0], bp[0], S[1]); VM(7); VC(6); LL(12); LL(13); LL(14); SB();
        VC(7); LL(15);
        S[1] = mfma(h1[1], bp[1], S[1]);
        asm volatile("s_cmp_lg_i32 %0, %1\n\ts_cbranch_scc1 1f\n\ts_setprio 0\n1:" ::"s"(T), "s"(tflip) : "scc");
        SB();
        z1p = z1;
        bp[0] = bn[0];
        bp[1] = bn[1];
        pp[0] = pn[0]; pp[1] = pn[1]; pp[2] = pn[2]; pp[3] = pn[3];
        aa[0] = an[0]; aa[1] = an[1];
      };
      const std::integral_constant<int, 0> B0;
      const std::integral_constant<int, 1> B1;
      partA(0);
      int t = 0;
      for (; t + 2 < ntiles; t += 2) {
        partB(t, B1);
        partA(t + 1);
        partB(t + 1, B0);
        partA(t + 2);
      }
      partB(t, B1);
      if (t + 1 < ntiles) {
        partA(t + 1);
        partB(t + 1, B0);
      }
      load_w2();
#pragma unroll
      for (int j = 0; j < 8; ++j) { VE(j); VA(j); VR(j); VM(j); VC(j); MG(j); }
      S[0] = mfma(h0[0], bp[0], S[0]);
      S[1] = mfma(h1[0], bp[0], S[1]);
      S[0] = mfma(h0[1], bp[1], S[0]);
      S[1] = mfma(h1[1], bp[1], S[1]);
      __builtin_amdgcn_s_setprio(0);
    }
    asm volatile("s_waitcnt vmcnt(0)" ::: "memory");
  } else {
    int round = 0;
    do {
      const unsigned lo = (unsigned)round * CAP;
      const unsigned rem = total > lo ? min(total - lo, (unsigned)CAP) : 0u;
      const __amdgpu_buffer_rsrc_t srdE = __builtin_amdgcn_make_buffer_rsrc((void*)eap, (short)0, (int)(rem * 8), 0x00020000);
      if (round > 0) {
        __syncthreads();
        for (int i = tt; i < 36 * 32; i += 256) lds_st<unsigned>(wbase + i * 4, PADWORD);
        for (int i = tt; i < 34 * 32; i += 256) lds_st<unsigned>(L_MASK + team * MASK_TEAM + i * 4, 0u);
        for (unsigned k = 0; k < segc; ++k) {
          const unsigned p = pos0 + k - lo;
          if (p < (unsigned)CAP) lds_st<unsigned>(IDX + p * 4, rec0 + k);
        }
        __syncthreads();
        fetch_records(rem);
#pragma unroll
        for (int k = 0; k < 4; ++k)
          if (tt + 256u * k < rem) place_at(tt + 256u * k, rec[k]);
        asm volatile("s_waitcnt vmcnt(0)" ::: "memory");
        __syncthreads();
        dmaP(0, lds_ld<u32x4>(w4a));
        dmaE(0, 0, srdE);
        dmaP(1, lds_ld<u32x4>(w4a + 128));
        dmaE(1, 1, srdE);
      }
      const int ntiles = (int)((rem + 31) >> 5);
      if (ntiles > 0) {
        u32x4 pp[4], aa[2];
        asm volatile("s_waitcnt vmcnt(5)" ::: "memory");
        load_ops(lds_ld<unsigned>(w1a), 0, pp, aa);
        {
          const u32x4 wd = lds_ld<u32x4>(w4a + 256);
          KEEP_OPS();
          dmaP(0, wd);
          dmaE(2, 0, srdE);
        }
        for (int t = 0; t < ntiles; ++t) {
          const int B = (t + 1) & 1;
          f32x16 z0 = mfma(aa[0], qf[0][0], zz), z1 = mfma(aa[0], qf[1][0], zz);
          z0 = mfma(aa[1], qf[0][1], z0);
          z1 = mfma(aa[1], qf[1][1], z1);
          z0 = mfma(pp[0], idn[0], z0);
          z1 = mfma(pp[2], idn[0], z1);
          z0 = mfma(pp[1], idn[1], z0);
          z1 = mfma(pp[3], idn[1], z1);
          u32x4 bb[2], h0[2], h1[2];
          bb[0] = lds_ld<u32x4>(L_BLUT + lds_ld<unsigned char>(mka + t * 128) * 16);
          bb[1] = lds_ld<u32x4>(L_BLUT + lds_ld<unsigned char>(mka + t * 128 + 2) * 16);
#pragma unroll
          for (int j = 0; j < 8; ++j) {
            const float za = z0[2 * j] * 64.0f, zb = z0[2 * j + 1] * 64.0f, ya = z1[2 * j], yb = z1[2 * j + 1];
            h0[j >> 2][j & 3] = pack_bf16(za * __builtin_amdgcn_rcpf(1.0f + __builtin_amdgcn_exp2f(-LOG2E * za)), zb * __builtin_amdgcn_rcpf(1.0f + __builtin_amdgcn_exp2f(-LOG2E * zb)));
            h1[j >> 2][j & 3] = pack_bf16(ya * __builtin_amdgcn_rcpf(1.0f + __builtin_amdgcn_exp2f(-ya)), yb * __builtin_amdgcn_rcpf(1.0f + __builtin_amdgcn_exp2f(-yb)));
          }
          S[0] = mfma(h0[0], bb[0], S[0]);
          S[1] = mfma(h1[0], bb[0], S[1]);
          S[0] = mfma(h0[1], bb[1], S[0]);
          S[1] = mfma(h1[1], bb[1], S[1]);
          asm volatile("s_waitcnt vmcnt(5)" ::: "memory");
          const u32x4 wd = lds_ld<u32x4>(w4a + (t + 3) * 128);
          load_ops(lds_ld<unsigned>(w1a + (t + 1) * 128), B, pp, aa);
          KEEP_OPS();
          dmaP(B, wd);
          dmaE(t + 3, B, srdE);
        }
      }
      asm volatile("s_waitcnt vmcnt(0)" ::: "memory");
    } while (++round < nrounds);
  }

  STAMP(16);
  CSTAMP(31);
  if (!w2_loaded) load_w2();
  __syncthreads();
  const unsigned EX = L_STG + team * 32768;
#pragma unroll
  for (int t = 0; t < 2; ++t) {
    const float sc = t ? LN2 : 1.0f;
#pragma unroll
    for (int s = 0; s < 2; ++s) {
      u32x4 v;
#pragma unroll
      for (int j = 0; j < 4; ++j) v[j] = pack_bf16(S[t][8 * s + 2 * j] * sc, S[t][8 * s + 2 * j + 1] * sc);
      lds_st<u32x4>(EX + (4 * w + 2 * t + s) * 1024 + lane * 16, v);
    }
  }
  __syncthreads();
  STAMP(17);
  const float deg = (float)lds_ld<unsigned>(L_CNT + team * 128 + r * 4);
  f32x16 o;
#pragma unroll
  for (int gq = 0; gq < 4; ++gq) {
    o[4 * gq + 0] = deg * __uint_as_float(b2v[gq][0] << 16);
    o[4 * gq + 1] = deg * __uint_as_float(b2v[gq][0] & 0xFFFF0000u);
    o[4 * gq + 2] = deg * __uint_as_float(b2v[gq][1] << 16);
    o[4 * gq + 3] = deg * __uint_as_float(b2v[gq][1] & 0xFFFF0000u);
  }
#pragma unroll
  for (int ks = 0; ks < 16; ++ks) o = mfma(w2f[ks], lds_ld<u32x4>(EX + ks * 1024 + lane * 16), o);
  if (r < G) {
    unsigned short* op = out + (size_t)(n0 + r) * 128 + 32 * w + 4 * h;
#pragma unroll
    for (int gq = 0; gq < 4; ++gq) {
      u32x2 v;
      v[0] = pack_bf16(o[4 * gq], o[4 * gq + 1]);
      v[1] = pack_bf16(o[4 * gq + 2], o[4 * gq + 3]);
      *(u32x2*)(op + 8 * gq) = v;
    }
  }
  STAMP(18);
}

}

extern "C" void kernel_launch(void* const* d_in, const int* in_sizes, int n_in, void* d_out, int out_size, void* d_ws,
                              size_t ws_size, hipStream_t stream) {
  const unsigned short* x = (const unsigned short*)d_in[0];
  const uint32_t* ea = (const uint32_t*)d_in[1];
  const int* src = (const int*)d_in[2];
  const int* dst = (const int*)d_in[3];
  const unsigned short* w1 = (const unsigned short*)d_in[4];
  const unsigned short* b1 = (const unsigned short*)d_in[5];
  const unsigned short* w2 = (const unsigned short*)d_in[6];
  const unsigned short* b2 = (const unsigned short*)d_in[7];
  (void)in_sizes; (void)n_in; (void)out_size; (void)ws_size;
  prep_kernel<<<PB_TBL, 512, 0, stream>>>(x, ea, src, dst, w1, w2, (char*)d_ws);
  main_kernel<<<MAIN_GRID, 512, 0, stream>>>(x, w1, b1, b2, (char*)d_ws, (unsigned short*)d_out);
}
```

```cpp
#include <hip/hip_runtime.h>
#include <hip/hip_bf16.h>
#include <stdint.h>
#include <type_traits>

namespace {

constexpr int NN = 12500;
constexpr int NE = 400000;
constexpr int NG = 512;
constexpr int NCH = 256;
constexpr int CH = 1563;
constexpr int CHP = 1568;
constexpr int CAP = 1024;

constexpr size_t WS_P = 4096;
constexpr size_t WS_REC = WS_P + 6400000;
constexpr size_t WS_TAB = WS_REC + (size_t)NCH * CHP * 12;
constexpr size_t WS_WB = WS_TAB + (size_t)NCH * NG * 4;
constexpr size_t WS_W2 = WS_WB + 65536;
constexpr size_t WS_TBL = WS_W2 + 65536;
constexpr size_t WS_MX = WS_TBL + 65536;
constexpr size_t WS_EA = WS_MX + 2048;
constexpr size_t WS_END = WS_EA + (size_t)NG * CAP * 8;
#define STAMP(k) do { } while (0)
#define CSTAMP(k) do { } while (0)
#define PSTAMP(k) do { } while (0)

constexpr int WRITE_THROUGH = 16;
constexpr float prio_rho = 0.4f;
#define MAIN_GRID 256
constexpr float LOG2E = 1.4426950408889634f;
constexpr float LN2 = 0.6931471805599453f;
constexpr float LSCALE = 1.0f / 64.0f;

typedef __attribute__((ext_vector_type(8))) short bf16x8;
typedef __attribute__((ext_vector_type(4))) short s16x4;
typedef __attribute__((ext_vector_type(16))) float f32x16;
typedef __attribute__((ext_vector_type(4))) unsigned u32x4;
typedef __attribute__((ext_vector_type(2))) unsigned u32x2;
typedef __attribute__((ext_vector_type(3))) unsigned u32x3;
typedef __attribute__((ext_vector_type(3), aligned(4))) unsigned u32x3_a4;
typedef __attribute__((ext_vector_type(2))) unsigned short u16x2;
typedef __bf16 bf16x2_t __attribute__((ext_vector_type(2)));
typedef float f32x2_t __attribute__((ext_vector_type(2)));
#define LDS_AS __attribute__((address_space(3)))
#define DI __device__ __forceinline__

DI unsigned pack_bf16(float a, float b) {
  f32x2_t v = {a, b};
  bf16x2_t r = __builtin_convertvector(v, bf16x2_t);
  return __builtin_bit_cast(unsigned, r);
}
DI float bf2f(unsigned short u) { return __uint_as_float((unsigned)u << 16); }
DI f32x16 mfma(u32x4 a, u32x4 b, f32x16 c) {
  return __builtin_amdgcn_mfma_f32_32x32x16_bf16(__builtin_bit_cast(bf16x8, a), __builtin_bit_cast(bf16x8, b), c, 0, 0, 0);
}
template <typename T> DI T lds_ld(unsigned addr) { return *(const LDS_AS T*)(size_t)addr; }
template <typename T> DI void lds_st(unsigned addr, T v) { *(LDS_AS T*)(size_t)addr = v; }
DI int crow(int i, int h) { return (i & 3) + 8 * (i >> 2) + 4 * h; }
DI unsigned wave_incl_scan(unsigned v) {
  v += __builtin_amdgcn_update_dpp(0u, v, 0x111, 0xF, 0xF, false);
  v += __builtin_amdgcn_update_dpp(0u, v, 0x112, 0xF, 0xF, false);
  v += __builtin_amdgcn_update_dpp(0u, v, 0x114, 0xF, 0xF, false);
  v += __builtin_amdgcn_update_dpp(0u, v, 0x118, 0xF, 0xF, false);
  v += __builtin_amdgcn_update_dpp(0u, v, 0x142, 0xA, 0xF, false);
  v += __builtin_amdgcn_update_dpp(0u, v, 0x143, 0xC, 0xF, false);
  return v;
}
DI unsigned wave_max_u32(unsigned v) {
  v = max(v, (unsigned)__builtin_amdgcn_update_dpp(0u, v, 0x111, 0xF, 0xF, false));
  v = max(v, (unsigned)__builtin_amdgcn_update_dpp(0u, v, 0x112, 0xF, 0xF, false));
  v = max(v, (unsigned)__builtin_amdgcn_update_dpp(0u, v, 0x114, 0xF, 0xF, false));
  v = max(v, (unsigned)__builtin_amdgcn_update_dpp(0u, v, 0x118, 0xF, 0xF, false));
  v = max(v, (unsigned)__builtin_amdgcn_update_dpp(0u, v, 0x142, 0xA, 0xF, false));
  v = max(v, (unsigned)__builtin_amdgcn_update_dpp(0u, v, 0x143, 0xC, 0xF, false));
  return v;
}
DI unsigned row_max_u32(unsigned v) {
  v = max(v, (unsigned)__builtin_amdgcn_update_dpp(0u, v, 0x111, 0xF, 0xF, false));
  v = max(v, (unsigned)__builtin_amdgcn_update_dpp(0u, v, 0x112, 0xF, 0xF, false));
  v = max(v, (unsigned)__builtin_amdgcn_update_dpp(0u, v, 0x114, 0xF, 0xF, false));
  v = max(v, (unsigned)__builtin_amdgcn_update_dpp(0u, v, 0x118, 0xF, 0xF, false));
  return v;
}
DI unsigned absbits(float f) { return __float_as_uint(f) & 0x7FFFFFFFu; }
DI void lds_max(unsigned addr, unsigned v) {
  __hip_atomic_fetch_max((LDS_AS unsigned*)(size_t)addr, v, __ATOMIC_RELAXED, __HIP_MEMORY_SCOPE_WORKGROUP);
}
DI int gstart(int g) { return (g * NN + (NG - 1)) / NG; }

constexpr int PB_T1 = 196;
constexpr int PB_T2 = PB_T1 + NCH;
constexpr int PB_WB = PB_T2 + 8;
constexpr int PB_W2 = PB_WB + 8;
constexpr int PB_TBL = PB_W2 + 8;
constexpr int PREP_LDS = 65536 + 64;

__global__ __launch_bounds__(512) void prep_kernel(const unsigned short* __restrict__ x, const uint32_t* __restrict__ ea,
                                                   const int* __restrict__ src, const int* __restrict__ dst,
                                                   const unsigned short* __restrict__ w1, const unsigned short* __restrict__ w2,
                                                   char* __restrict__ ws) {
  __shared__ __attribute__((aligned(16))) char lds[PREP_LDS];
  const unsigned lb = (unsigned)(size_t)lds;
  const int tid = threadIdx.x, lane = tid & 63, wv = tid >> 6;
  const int blk = blockIdx.x;
  PSTAMP(0);
  if (blk < PB_T1) {
    const int n0 = (blk >> 1) * 128, half = blk & 1;
    const unsigned XO = lb, WO = lb + 32768;
    const __amdgpu_buffer_rsrc_t srdPw = __builtin_amdgcn_make_buffer_rsrc((void*)(ws + WS_P), (short)0, NN * 512, 0x00020000);
    u32x4 xv[4], wv4[4];
#pragma unroll
    for (int k = 0; k < 4; ++k) {
      const int q = tid + 512 * k, row = q >> 4, ch = q & 15;
      const int n = min(n0 + row, NN - 1);
      xv[k] = *(const u32x4*)(x + (size_t)n * 128 + ch * 8);
      wv4[k] = *(const u32x4*)(w1 + (size_t)row * 256 + half * 128 + ch * 8);
    }
#pragma unroll
    for (int k = 0; k < 4; ++k) {
      const int q = tid + 512 * k, row = q >> 4, ch = q & 15;
      lds_st<u32x4>(XO + row * 256 + ((ch ^ (row & 15)) << 4), xv[k]);
      lds_st<u32x4>(WO + row * 256 + ((ch ^ (((row & 3) << 2) | ((row >> 2) & 3))) << 4), wv4[k]);
    }
    if (tid == 0) lds_st<unsigned>(lb + 65536, 0u);
    __syncthreads();
    PSTAMP(1);
    const int nt = wv & 3, cp = wv >> 2;
    const int r = lane & 31, h = lane >> 5, bk = (lane >> 4) & 1, q4 = (lane & 15) >> 2, p4 = lane & 3;
    f32x16 acc[2];
#pragma unroll
    for (int i = 0; i < 16; ++i) { acc[0][i] = 0.f; acc[1][i] = 0.f; }
    const int xrow = 32 * nt + r;
    const unsigned xb = XO + xrow * 256 + ((h ^ (xrow & 15)) << 4);
    const int chw = 8 * cp + 2 * bk + (p4 >> 1), mA = (q4 << 2) | (2 * h), mB = mA | 1;
    const unsigned wa0 = WO + (8 * h + q4) * 256 + ((chw ^ mA) << 4) + 8 * (p4 & 1);
    const unsigned wa1 = WO + (8 * h + q4 + 4) * 256 + ((chw ^ mB) << 4) + 8 * (p4 & 1);
#pragma unroll
    for (int ks = 0; ks < 8; ++ks) {
      const u32x4 bfr = lds_ld<u32x4>(xb ^ (unsigned)(ks << 5));
#pragma unroll
      for (int ct = 0; ct < 2; ++ct) {
        const s16x4 lo = __builtin_amdgcn_ds_read_tr16_b64_v4i16((LDS_AS s16x4*)(size_t)((wa0 ^ (unsigned)(ct << 6)) + ks * 4096));
        const s16x4 hi = __builtin_amdgcn_ds_read_tr16_b64_v4i16((LDS_AS s16x4*)(size_t)((wa1 ^ (unsigned)(ct << 6)) + ks * 4096));
        const bf16x8 afr = __builtin_shufflevector(lo, hi, 0, 1, 2, 3, 4, 5, 6, 7);
        acc[ct] = mfma(__builtin_bit_cast(u32x4, afr), bfr, acc[ct]);
      }
    }
    __syncthreads();
    PSTAMP(2);
#pragma unroll
    for (int ct = 0; ct < 2; ++ct) {
      const float sc = ct ? LOG2E : LSCALE;
#pragma unroll
      for (int g = 0; g < 4; ++g) {
        u32x2 pk;
        pk[0] = pack_bf16(acc[ct][4 * g] * sc, acc[ct][4 * g + 1] * sc);
        pk[1] = pack_bf16(acc[ct][4 * g + 2] * sc, acc[ct][4 * g + 3] * sc);
        lds_st<u32x2>(lb + xrow * 272 + (64 * cp + 32 * ct + 8 * g + 4 * h) * 2, pk);
      }
    }
    __syncthreads();
#pragma unroll
    for (int k = 0; k < 4; ++k) {
      const int q = tid + 512 * k, row = q >> 4, ch = q & 15;
      __builtin_amdgcn_raw_buffer_store_b128(lds_ld<u32x4>(lb + row * 272 + (ch << 4)), srdPw, (n0 + row) * 512 + half * 256 + ch * 16, 0, WRITE_THROUGH);
    }
    {
      int mi = 0;
      unsigned mu = 0;
#pragma unroll
      for (int i = 0; i < 16; ++i) {
        const unsigned a = __float_as_uint(acc[0][i]), b = __float_as_uint(acc[1][i]);
        mi = max(mi, max((int)a, (int)b));
        mu = max(mu, max(a, b));
      }
      const unsigned m = wave_max_u32(max((unsigned)mi, mu & 0x7FFFFFFFu));
      if (lane == 63) lds_max(lb + 65536, m);
    }
    __syncthreads();
    if (tid == 0) ((unsigned*)(ws + WS_MX))[blk] = lds_ld<unsigned>(lb + 65536);
  } else if (blk < PB_T2) {
    const int bi = blk - PB_T1;
    const int b = ((blk & 7) << 5) + (bi >> 3);
    const unsigned HO = lb, SO = lb + 2048, RO = lb + 4096;
    const __amdgpu_buffer_rsrc_t srdRw = __builtin_amdgcn_make_buffer_rsrc((void*)(ws + WS_REC + (size_t)b * CHP * 12), (short)0, CHP * 12, 0x00020000);
    lds_st<unsigned>(HO + tid * 4, 0u);
    if (tid == 0) lds_st<unsigned>(SO + 128, 0u);
    int dv[4], sv[4];
    u32x2 ev[4];
#pragma unroll
    for (int k = 0; k < 4; ++k) {
      const int i = tid + 512 * k, e = b * CH + i;
      const bool ok = i < CH && e < NE;
      const int ec = ok ? e : 0;
      dv[k] = dst[ec];
      sv[k] = src[ec];
      ev[k] = *(const u32x2*)(ea + (size_t)ec * 2);
      if (!ok) dv[k] = -1;
    }
    __syncthreads();
    PSTAMP(1);
    int gk[4];
    unsigned rk[4];
#pragma unroll
    for (int k = 0; k < 4; ++k) {
      gk[k] = (int)((float)dv[k] * ((float)NG / (float)NN) + 1e-4f);
      rk[k] = __hip_atomic_fetch_add((LDS_AS unsigned*)(size_t)(dv[k] >= 0 ? HO + gk[k] * 4 : SO + 64), 1u, __ATOMIC_RELAXED, __HIP_MEMORY_SCOPE_WORKGROUP);
    }
    __syncthreads();
    PSTAMP(2);
    const unsigned c = lds_ld<unsigned>(HO + tid * 4);
    const unsigned inc = wave_incl_scan(c);
    if (lane == 63) lds_st<unsigned>(SO + wv * 4, inc);
    __syncthreads();
    const u32x4 t0 = lds_ld<u32x4>(SO), t1 = lds_ld<u32x4>(SO + 16);
    unsigned pre = 0;
    pre += wv > 0 ? t0[0] : 0u; pre += wv > 1 ? t0[1] : 0u; pre += wv > 2 ? t0[2] : 0u; pre += wv > 3 ? t0[3] : 0u;
    pre += wv > 4 ? t1[0] : 0u; pre += wv > 5 ? t1[1] : 0u; pre += wv > 6 ? t1[2] : 0u;
    const unsigned start = pre + inc - c;
    ((uint32_t*)(ws + WS_TAB))[tid * NCH + b] = start | (c << 16);
    lds_st<unsigned>(HO + tid * 4, start);
    __syncthreads();
#pragma unroll
    for (int k = 0; k < 4; ++k) {
      if (dv[k] >= 0) {
        const unsigned st = lds_ld<unsigned>(HO + gk[k] * 4);
        const unsigned ra = RO + (st + rk[k]) * 12;
        lds_st<unsigned>(ra, ((unsigned)sv[k] << 9) | (((unsigned)(dv[k] * NG - gk[k] * NN) >> 5) & 0x1F0u));
        lds_st<unsigned>(ra + 4, ev[k][0]);
        lds_st<unsigned>(ra + 8, ev[k][1]);
      }
    }
    __syncthreads();
#pragma unroll
    for (int k = 0; k < 3; ++k) {
      const int i = tid + 512 * k;
      if (i < CHP * 12 / 16) __builtin_amdgcn_raw_buffer_store_b128(lds_ld<u32x4>(RO + i * 16), srdRw, i * 16, 0, WRITE_THROUGH);
    }
    {
      u16x2 pm = {0, 0};
#pragma unroll
      for (int k = 0; k < 4; ++k) {
        pm = __builtin_elementwise_max(pm, __builtin_bit_cast(u16x2, ev[k][0] & 0x7FFF7FFFu));
        pm = __builtin_elementwise_max(pm, __builtin_bit_cast(u16x2, ev[k][1] & 0x7FFF7FFFu));
      }
      const unsigned m = wave_max_u32((unsigned)max(pm[0], pm[1]) << 16);
      if (lane == 63) lds_max(SO + 128, m);
    }
    __syncthreads();
    if (tid == 0) ((unsigned*)(ws + WS_MX))[256 + b] = lds_ld<unsigned>(SO + 128);
  } else if (blk < PB_WB) {
    const int q = (blk - PB_T2) * 512 + tid;
    const int l = q & 63, ks = (q >> 6) & 7, ct = q >> 9;
    const int col = 32 * ct + (l & 31), k0 = 128 + 16 * ks + 8 * (l >> 5);
    unsigned short v[8];
#pragma unroll
    for (int j = 0; j < 8; ++j) v[j] = w1[(size_t)(k0 + j) * 256 + col];
    u32x4 o;
#pragma unroll
    for (int j = 0; j < 4; ++j) o[j] = v[2 * j] | ((unsigned)v[2 * j + 1] << 16);
    *(u32x4*)(ws + WS_WB + (size_t)q * 16) = o;
  } else if (blk >= PB_W2) {
    const unsigned c0 = (unsigned)((blk - PB_W2) * 512 + tid) * 4;
    u32x2 tp, tn;
#pragma unroll
    for (int j = 0; j < 2; ++j) {
      const unsigned b0 = ((c0 + 2 * j) << 16) | 0x8000u;
      const float za = __uint_as_float(b0) * 64.0f, zb = __uint_as_float(b0 + 0x10000u) * 64.0f;
      const float ea_ = __builtin_amdgcn_exp2f(-LOG2E * za), eb_ = __builtin_amdgcn_exp2f(-LOG2E * zb);
      const float ra = __builtin_amdgcn_rcpf(1.0f + ea_), rb = __builtin_amdgcn_rcpf(1.0f + eb_);
      tp[j] = pack_bf16(za * ra, zb * rb);
      tn[j] = pack_bf16(-(za * ea_) * ra, -(zb * eb_) * rb);
    }
    *(u32x2*)(ws + WS_TBL + c0 * 2) = tp;
    *(u32x2*)(ws + WS_TBL + 32768 + c0 * 2) = tn;
  } else {
    const int q = (blk - PB_WB) * 512 + tid;
    const int l = q & 63, ot = (q >> 6) & 3, ks = q >> 8;
    const int col = 32 * ot + (l & 31), h = l >> 5;
    unsigned short v[8];
#pragma unroll
    for (int j = 0; j < 8; ++j) v[j] = w2[(size_t)(32 * (ks >> 1) + crow(8 * (ks & 1) + j, h)) * 128 + col];
    u32x4 o;
#pragma unroll
    for (int j = 0; j < 4; ++j) o[j] = v[2 * j] | ((unsigned)v[2 * j + 1] << 16);
    *(u32x4*)(ws + WS_W2 + (size_t)q * 16) = o;
  }
  PSTAMP(3);
}

constexpr unsigned L_TP = 0;
constexpr unsigned L_WORD = 32768;
constexpr unsigned WORD_TEAM = 36 * 128;
constexpr unsigned L_MASK = L_WORD + 2 * WORD_TEAM;
constexpr unsigned MASK_TEAM = 34 * 128;
constexpr unsigned L_CNT = L_MASK + 2 * MASK_TEAM;
constexpr unsigned L_TOT = L_CNT + 256;
constexpr unsigned L_MAX = L_TOT + 16;
constexpr unsigned L_BKT = L_TOT + 64;
constexpr unsigned L_ZEND = L_TOT + 128;
constexpr unsigned L_ALUT = (L_ZEND + 511) / 512 * 512;
constexpr unsigned L_BLUT = L_ALUT + 2048;
constexpr unsigned L_EA = L_BLUT + 4096;
constexpr unsigned L_AEND = L_EA + 4096;
constexpr unsigned L_TN = 65536;
constexpr unsigned L_STG = 98304;
constexpr unsigned L_END = 163840;
static_assert(L_AEND <= 65536 && (L_ZEND - L_MASK) % 16 == 0 && L_ALUT % 512 == 0, "LDS region A");
constexpr unsigned PADWORD = 0x7FFFFE00u | (31u << 4);

__global__ __launch_bounds__(512, 2) void main_kernel(const unsigned short* __restrict__ x, const unsigned short* __restrict__ w1,
                                                      const unsigned short* __restrict__ b1, const unsigned short* __restrict__ b2,
                                                      char* __restrict__ ws, unsigned short* __restrict__ out) {
  __shared__ __attribute__((aligned(16))) char lds[L_END];
  asm volatile("" ::"s"((unsigned)(size_t)lds));
  const int tid = threadIdx.x, lane = tid & 63;
  const int wv = __builtin_amdgcn_readfirstlane(tid >> 6);
  const int team = wv >> 2, w = wv & 3, tt = tid & 255;
  const int wg = (blockIdx.x & 7) * 32 + (blockIdx.x >> 3);
  const int g = 2 * wg + team;
  const int n0 = gstart(g), G = gstart(g + 1) - n0;
  const int r = lane & 31, h = lane >> 5;

  const __amdgpu_buffer_rsrc_t srdP = __builtin_amdgcn_make_buffer_rsrc((void*)(ws + WS_P - 3072), (short)0, NN * 512 + 3072, 0x00020000);
  STAMP(0);
  const uint32_t tabv = ((const uint32_t*)(ws + WS_TAB))[g * NCH + tt];
  u32x4 xs[2];
#pragma unroll
  for (int k = 0; k < 2; ++k) {
    const int q = tt + 256 * k, row = q >> 4, ch = q & 15;
    const int n = min(n0 + row, NN - 1);
    xs[k] = *(const u32x4*)(x + (size_t)n * 128 + ch * 8);
  }
  unsigned mxv = ((const unsigned*)(ws + WS_MX))[tid];
  const unsigned short cb0 = b1[32 * wv + r];
  const unsigned short cb1 = w1[(size_t)(256 + 2 * h) * 256 + 32 * wv + r];
  const unsigned short cb2 = w1[(size_t)(257 + 2 * h) * 256 + 32 * wv + r];

  STAMP(1);
  {
    const u32x4 pad = {PADWORD, PADWORD, PADWORD, PADWORD}, zero = {0, 0, 0, 0};
    lds_st<u32x4>(L_WORD + tid * 16, pad);
    if (tid < (int)(2 * WORD_TEAM / 16 - 512)) lds_st<u32x4>(L_WORD + (512 + tid) * 16, pad);
    lds_st<u32x4>(L_MASK + tid * 16, zero);
    if (tid < (int)((L_ZEND - L_MASK) / 16 - 512)) lds_st<u32x4>(L_MASK + (512 + tid) * 16, zero);
  }
  if (tid < 128) {
    const int u = tid & 31, hh = (tid >> 5) & 1, s = tid >> 6;
    const int rho = u < 26 ? u : u + 2;
    const int i = (rho & 3) + 4 * (rho >> 3), j = i & 7;
    const bool on = u < 28 && ((rho >> 2) & 1) == hh && (i >> 3) == s;
    const unsigned one = on ? (0x3F80u << (16 * (j & 1))) : 0u;
    u32x4 v;
#pragma unroll
    for (int q = 0; q < 4; ++q) v[q] = (j >> 1) == q ? one : 0u;
    lds_st<u32x4>(L_ALUT + tid * 16, v);
  } else if (tid < 384) {
    const int bb = tid - 128;
    u32x4 v;
#pragma unroll
    for (int j = 0; j < 4; ++j) v[j] = ((bb >> (2 * j)) & 1 ? 0x3F80u : 0u) | ((bb >> (2 * j + 1)) & 1 ? 0x3F800000u : 0u);
    lds_st<u32x4>(L_BLUT + bb * 16, v);
  }
  STAMP(2);
  u32x4 wb[8];
#pragma unroll
  for (int k = 0; k < 8; ++k) wb[k] = *(const u32x4*)(ws + WS_WB + (size_t)(wv * 8 + k) * 1024 + lane * 16);

  const unsigned wbase = L_WORD + team * WORD_TEAM;
  const unsigned stg = L_STG + wv * 8192;
  const unsigned lc0 = (unsigned)(w * 128 + (((lane & 7) ^ ((lane >> 4) & 3)) << 4));
  const unsigned lc1 = lc0 ^ 64u;
  const unsigned w4a = wbase + (lane >> 3) * 16;
  const unsigned eav = (unsigned)((2 * r + h) * 4);
  STAMP(3);

  auto xaddr = [&](int tm, int row, int ch) { return L_STG + (unsigned)(2 * tm + (row >> 4)) * 8192 + (unsigned)(row & 15) * 256 + (unsigned)((ch ^ (row & 15)) << 4); };
  auto qaddr = [&](int tm, int ct, int s) { const int i = (tm * 8 + ct) * 2 + s; return L_STG + (unsigned)(i >> 2) * 8192 + 4096 + (unsigned)(i & 3) * 1024 + lane * 16; };
  const unsigned IDX = L_STG + (4 + team) * 8192;
#pragma unroll
  for (int k = 0; k < 2; ++k) {
    const int q = tt + 256 * k, row = q >> 4, ch = q & 15;
    u32x4 v = xs[k];
    if (row >= G) v = u32x4{0, 0, 0, 0};
    lds_st<u32x4>(xaddr(team, row, ch), v);
  }
  STAMP(4);
  __syncthreads();

  STAMP(5);
  const unsigned segs = tabv & 0xFFFFu, segc = tabv >> 16;
  const unsigned inc = wave_incl_scan(segc);
  unsigned wbase0 = 0;
  if (lane == 63) wbase0 = __hip_atomic_fetch_add((LDS_AS unsigned*)(size_t)(L_TOT + team * 4), inc, __ATOMIC_RELAXED, __HIP_MEMORY_SCOPE_WORKGROUP);
  const unsigned pos0 = __builtin_amdgcn_readlane(wbase0, 63) + inc - segc;
  const unsigned rec0 = (unsigned)tt * CHP + segs;
  char* eap = ws + WS_EA + (size_t)g * CAP * 8;
  {
    const unsigned kend = pos0 < (unsigned)CAP ? min(segc, (unsigned)CAP - pos0) : 0u;
    for (unsigned k = 0; k < kend; ++k) lds_st<unsigned>(IDX + (pos0 + k) * 4, rec0 + k);
  }
  STAMP(6);
  __syncthreads();

  unsigned total, tot0;
  int nrounds;
  {
    const unsigned t0 = lds_ld<unsigned>(L_TOT), t1 = lds_ld<unsigned>(L_TOT + 4);
    tot0 = __builtin_amdgcn_readfirstlane(t0);
    total = __builtin_amdgcn_readfirstlane(team ? t1 : t0);
    nrounds = __builtin_amdgcn_readfirstlane((int)((max(t0, t1) + CAP - 1) / CAP));
  }
  STAMP(7);
  u32x3 rec[4];
  auto fetch_records = [&](unsigned rem) {
#pragma unroll
    for (int k = 0; k < 4; ++k) {
      const unsigned p = tt + 256 * k;
      const unsigned ri = lds_ld<unsigned>(IDX + p * 4);
      rec[k] = *(const u32x3_a4*)(ws + WS_REC + (size_t)(p < rem ? ri : 0u) * 12);
    }
  };
  fetch_records(min(total, (unsigned)CAP));
  STAMP(8);
  {
    const int u = r < 26 ? r : (r == 28 || r == 29) ? r - 2 : 31;
    const float sc = (wv & 1) ? LOG2E : LSCALE;
    const float c0s = bf2f(cb0) * sc, cin1 = bf2f(cb1), cin2 = bf2f(cb2);
    const unsigned xa = xaddr(0, u, h);
    f32x16 acc[2];
    u32x4 af[2][8];
#pragma unroll
    for (int tm = 0; tm < 2; ++tm) {
#pragma unroll
      for (int ks = 0; ks < 8; ++ks) af[tm][ks] = lds_ld<u32x4>((xa ^ (unsigned)(ks << 5)) + tm * 16384);
#pragma unroll
      for (int i = 0; i < 16; ++i) acc[tm][i] = 0.f;
    }
    __builtin_amdgcn_sched_barrier(0);
#pragma unroll
    for (int ks = 0; ks < 8; ++ks) {
      acc[0] = mfma(af[0][ks], wb[ks], acc[0]);
      acc[1] = mfma(af[1][ks], wb[ks], acc[1]);
    }
    int mi = 0;
    unsigned mu = 0;
    const unsigned wrow = pack_bf16(cin1 * sc, cin2 * sc);
#pragma unroll
    for (int tm = 0; tm < 2; ++tm) {
      float q[14];
#pragma unroll
      for (int i = 0; i < 14; ++i) q[i] = __builtin_fmaf(acc[tm][i], sc, c0s);
#pragma unroll
      for (int i = 0; i < 14; i += 2) {
        const unsigned a = __float_as_uint(q[i]), b = __float_as_uint(q[i + 1]);
        mi = max(mi, max((int)a, (int)b));
        mu = max(mu, max(a, b));
      }
      lds_st<u32x4>(qaddr(tm, wv, 0), u32x4{pack_bf16(q[0], q[1]), pack_bf16(q[2], q[3]), pack_bf16(q[4], q[5]), pack_bf16(q[6], q[7])});
      lds_st<u32x4>(qaddr(tm, wv, 1), u32x4{pack_bf16(q[8], q[9]), pack_bf16(q[10], q[11]), pack_bf16(q[12], q[13]), wrow});
    }
    const unsigned mq = row_max_u32(__float_as_uint(__uint_as_float(max((unsigned)mi, mu & 0x7FFFFFFFu)) * (1.0f / sc)));
    const unsigned mw = row_max_u32(max(absbits(cin1), absbits(cin2)));
    const unsigned mx = row_max_u32(tid < PB_T1 || tid >= 256 ? mxv : 0u);
    if ((lane & 15) == 15)
      asm volatile("ds_max_u32 %0, %1\n\tds_max_u32 %2, %3 offset:8\n\tds_max_u32 %2, %4 offset:12" ::"v"(L_MAX + (team ? 4u : 0u)), "v"(mx), "v"(L_MAX), "v"(mq), "v"(mw) : "memory");
  }
  {
    const __amdgpu_buffer_rsrc_t srdT = __builtin_amdgcn_make_buffer_rsrc((void*)(ws + WS_TBL), (short)0, 65536, 0x00020000);
    const unsigned vt = (unsigned)(wv * 8192 + lane * 16), mt = (wv < 4 ? L_TP : L_TN - 32768) + wv * 8192;
    asm volatile(
        "s_mov_b32 m0, %1\n\ts_nop 0\n\tbuffer_load_dwordx4 %0, %3, 0 offen lds\n\t"
        "buffer_load_dwordx4 %0, %3, 0 offen offset:1024 lds\n\t"
        "buffer_load_dwordx4 %0, %3, 0 offen offset:2048 lds\n\t"
        "buffer_load_dwordx4 %0, %3, 0 offen offset:3072 lds\n\t"
        "s_mov_b32 m0, %2\n\ts_nop 0\n\tbuffer_load_dwordx4 %0, %3, %4 offen lds\n\t"
        "buffer_load_dwordx4 %0, %3, %4 offen offset:1024 lds\n\t"
        "buffer_load_dwordx4 %0, %3, %4 offen offset:2048 lds\n\t"
        "buffer_load_dwordx4 %0, %3, %4 offen offset:3072 lds"
        ::"v"(vt), "s"(mt), "s"(mt + 4096), "s"(srdT), "s"(4096)
        : "memory");
  }
  STAMP(9);
  f32x16 S[2];
#pragma unroll
  for (int i = 0; i < 16; ++i) { S[0][i] = 0.f; S[1][i] = 0.f; }
  auto place_at = [&](unsigned p, const u32x3& rc) {
    const unsigned tile = p >> 5, rr = p & 31;
    lds_st<unsigned>(wbase + tile * 128 + ((rr & 7) * 4 + (rr >> 3)) * 4, rc[0]);
    const unsigned u = (rc[0] >> 4) & 31;
    const unsigned bit = 16 * (rr >> 4) + 8 * ((rr >> 2) & 1) + 4 * ((rr >> 3) & 1) + (rr & 3);
    __hip_atomic_fetch_or((LDS_AS unsigned*)(size_t)(L_MASK + team * MASK_TEAM + tile * 128 + u * 4), 1u << bit, __ATOMIC_RELAXED, __HIP_MEMORY_SCOPE_WORKGROUP);
    __hip_atomic_fetch_add((LDS_AS unsigned*)(size_t)(L_CNT + team * 128 + u * 4), 1u, __ATOMIC_RELAXED, __HIP_MEMORY_SCOPE_WORKGROUP);
    *(u32x2*)(eap + (size_t)p * 8) = u32x2{rc[1], rc[2]};
  };
  const unsigned rem0 = min(total, (unsigned)CAP);
  {
    unsigned bk[4], rk[4];
#pragma unroll
    for (int k = 0; k < 4; ++k) {
      bk[k] = rec[k][0] >> 20;
      rk[k] = 0;
      if (tt + 256u * k < rem0) rk[k] = __hip_atomic_fetch_add((LDS_AS unsigned*)(size_t)(L_BKT + team * 32 + bk[k] * 4), 1u, __ATOMIC_RELAXED, __HIP_MEMORY_SCOPE_WORKGROUP);
    }
    asm volatile("s_waitcnt lgkmcnt(0)" ::: "memory");
    __builtin_amdgcn_s_barrier();
    const unsigned cnt8 = lds_ld<unsigned>(L_BKT + team * 32 + (lane & 7) * 4);
    const unsigned pre8 = wave_incl_scan(lane < 8 ? cnt8 : 0u) - cnt8;
#pragma unroll
    for (int k = 0; k < 4; ++k) {
      const unsigned base = __builtin_amdgcn_ds_bpermute(bk[k] * 4, pre8);
      if (tt + 256u * k < rem0) place_at(base + rk[k], rec[k]);
    }
  }
  STAMP(10);
  asm volatile("s_waitcnt lgkmcnt(0)" ::: "memory");
  __builtin_amdgcn_s_barrier();
  STAMP(11);

  auto dmaP = [&](int B, u32x4 wd) {
    const unsigned v0 = (wd[0] & 0xFFFFFE00u) | lc0, v1 = (wd[1] & 0xFFFFFE00u) | lc1;
    const unsigned v2 = (wd[2] & 0xFFFFFE00u) | lc0, v3 = (wd[3] & 0xFFFFFE00u) | lc1;
    const unsigned m0p = stg + B * 4096;
    asm volatile(
        "s_mov_b32 m0, %4\n\ts_nop 0\n\tbuffer_load_dwordx4 %0, %5, %6 offen lds\n\t"
        "buffer_load_dwordx4 %1, %5, %7 offen offset:1024 lds\n\t"
        "buffer_load_dwordx4 %2, %5, %8 offen offset:2048 lds\n\t"
        "buffer_load_dwordx4 %3, %5, 0 offen offset:3072 lds"
        ::"v"(v0), "v"(v1), "v"(v2), "v"(v3), "s"(m0p), "s"(srdP), "s"(3072), "s"(2048), "s"(1024)
        : "memory");
  };
  auto dmaE = [&](int T, int B, __amdgpu_buffer_rsrc_t srdE) {
    const unsigned ve = eav + (unsigned)T * 256u;
    const unsigned m0e = L_EA + wv * 512 + B * 256;
    asm volatile("s_mov_b32 m0, %1\n\ts_nop 0\n\tbuffer_load_dword %0, %2, 0 offen lds" ::"v"(ve), "s"(m0e), "s"(srdE) : "memory");
  };
  const __amdgpu_buffer_rsrc_t srdE0 = __builtin_amdgcn_make_buffer_rsrc((void*)eap, (short)0, (int)(rem0 * 8), 0x00020000);
  dmaP(0, lds_ld<u32x4>(w4a));

  u32x4 qf[2][2];
#pragma unroll
  for (int t = 0; t < 2; ++t)
#pragma unroll
    for (int s = 0; s < 2; ++s) qf[t][s] = lds_ld<u32x4>(qaddr(team, 2 * w + t, s));
  bool fast;
  {
    const u32x4 mx = lds_ld<u32x4>(L_MAX);
    const float bound = __uint_as_float(mx[0]) + __uint_as_float(mx[2]) + 4.0f * __uint_as_float(mx[1]) * __uint_as_float(mx[3]);
    fast = __builtin_amdgcn_readfirstlane(bound < 120.0f ? 1 : 0) != 0 && nrounds <= 1;
  }
  asm volatile("" ::"v"(qf[0][0]), "v"(qf[0][1]), "v"(qf[1][0]), "v"(qf[1][1]));
  STAMP(19);
  u32x4 idn[2];
#pragma unroll
  for (int s = 0; s < 2; ++s) {
    const unsigned d = (unsigned)(r - 16 * s - 8 * h);
    idn[s] = lds_ld<u32x4>(L_BLUT + (d < 8u ? (16u << d) : 0u));
  }
  const unsigned pb = (unsigned)(r * 128 + ((h ^ ((r >> 1) & 7)) << 4));
  unsigned pa0 = stg + pb, pa1 = stg + (pb ^ 32u), pa2 = stg + (pb ^ 64u), pa3 = stg + (pb ^ 96u);
  unsigned w1a = wbase + ((r & 7) * 4 + (r >> 3)) * 4;
  unsigned mka = L_MASK + team * MASK_TEAM + r * 4 + h;
  unsigned eal = L_EA + wv * 512 + lane * 4;
  unsigned alh = L_ALUT + h * 512;
  asm volatile("" : "+v"(idn[0]), "+v"(idn[1]), "+v"(pa0), "+v"(pa1), "+v"(pa2), "+v"(pa3), "+v"(w1a), "+v"(mka), "+v"(eal), "+v"(alh));
  {
    const u32x4 wd_1 = lds_ld<u32x4>(w4a + 128);
    STAMP(20);
    asm volatile("s_waitcnt vmcnt(4) lgkmcnt(0)" ::: "memory");
    STAMP(21);
    __builtin_amdgcn_s_barrier();
    dmaE(0, 0, srdE0);
    dmaP(1, wd_1);
    dmaE(1, 1, srdE0);
  }
  STAMP(12);
  auto load_ops = [&](unsigned wd, int B, u32x4 (&pp)[4], u32x4 (&aa)[2]) {
    const unsigned ad = (wd & 0x1F0u) | alh;
    aa[0] = lds_ld<u32x4>(ad);
    const u32x3 a1 = lds_ld<u32x3>(ad + 1024);
    aa[1][0] = a1[0]; aa[1][1] = a1[1]; aa[1][2] = a1[2];
    aa[1][3] = lds_ld<unsigned>(eal + B * 256);
    pp[0] = lds_ld<u32x4>(pa0 + B * 4096);
    pp[1] = lds_ld<u32x4>(pa1 + B * 4096);
    pp[2] = lds_ld<u32x4>(pa2 + B * 4096);
    pp[3] = lds_ld<u32x4>(pa3 + B * 4096);
  };
#define SB() __builtin_amdgcn_sched_barrier(0)
#define KEEP_OPS() asm volatile("" ::"v"(pp[0]), "v"(pp[1]), "v"(pp[2]), "v"(pp[3]), "v"(aa[1]))
  f32x16 zz;
#pragma unroll
  for (int i = 0; i < 16; ++i) zz[i] = 0.f;

  u32x4 w2f[16];
  u32x2 b2v[4];
  bool w2_loaded = false;
  auto load_w2 = [&]() {
#pragma unroll
    for (int ks = 0; ks < 16; ++ks) w2f[ks] = *(const u32x4*)(ws + WS_W2 + (size_t)(ks * 4 + w) * 1024 + lane * 16);
#pragma unroll
    for (int gq = 0; gq < 4; ++gq) b2v[gq] = *(const u32x2*)(b2 + 32 * w + 8 * gq + 4 * h);
    w2_loaded = true;
  };
  if (fast) {
    const int ntiles = (int)((rem0 + 31) >> 5);
    if (ntiles > 0) {
      u32x4 pp[4], aa[2];
      asm volatile("s_waitcnt vmcnt(5)" ::: "memory");
      STAMP(14);
      load_ops(lds_ld<unsigned>(w1a), 0, pp, aa);
      {
        const u32x4 wd = lds_ld<u32x4>(w4a + 256);
        KEEP_OPS();
        dmaP(0, wd);
        dmaE(2, 0, srdE0);
      }
      const float n0f = (float)((min(tot0, (unsigned)CAP) + 31) >> 5), n1f = (float)ntiles;
      const int tflip = __builtin_amdgcn_readfirstlane(team ? (int)((n1f - prio_rho * n0f) * (1.0f / (1.0f - prio_rho * prio_rho))) : -1);
      asm volatile("s_cmp_lt_i32 %0, 1\n\ts_cbranch_scc1 1f\n\ts_setprio 1\n1:" ::"s"(tflip) : "scc");
      STAMP(15);
      CSTAMP(30);
      u32x4 bp[2], bn[2], h0[2], h1[2], wd4;
      f32x16 z0, z1, z1p;
      unsigned lk[16], mb0, mb1, wd1;
      f32x2_t E[8], R[8], M[8];
#pragma unroll
      for (int i = 0; i < 16; ++i) { z1p[i] = 0.f; lk[i] = 0; }
      bp[0] = u32x4{0, 0, 0, 0};
      bp[1] = bp[0];
#define VE(j) E[j] = f32x2_t{__builtin_amdgcn_exp2f(-z1p[2 * (j)]), __builtin_amdgcn_exp2f(-z1p[2 * (j) + 1])}
#define VA(j) E[j] = E[j] + 1.0f
#define VR(j) R[j] = f32x2_t{__builtin_amdgcn_rcpf(E[j][0]), __builtin_amdgcn_rcpf(E[j][1])}
#define VM(j) M[j] = f32x2_t{z1p[2 * (j)], z1p[2 * (j) + 1]} * R[j]
#define VC(j) h1[(j) >> 2][(j) & 3] = pack_bf16(M[j][0], M[j][1])
#define MG(j) h0[(j) >> 2][(j) & 3] = lk[2 * (j)] | (lk[2 * (j) + 1] << 16)
#define LL(i) lk[i] = lds_ld<unsigned short>((__float_as_uint(z0[i]) >> 16) << 1)
      auto partA = [&](int T) {
        z0 = mfma(aa[0], qf[0][0], zz); VE(0); MG(0); SB();
        z0 = mfma(aa[1], qf[0][1], z0); VE(1); VA(0); MG(1); SB();
        z0 = mfma(pp[0], idn[0], z0); VE(2); VA(1); VR(0); MG(2);
        mb0 = lds_ld<unsigned char>(mka + T * 128); mb1 = lds_ld<unsigned char>(mka + T * 128 + 2);
        wd1 = lds_ld<unsigned>(w1a + (T + 1) * 128); wd4 = lds_ld<u32x4>(w4a + (T + 3) * 128);
        SB();
        z0 = mfma(pp[1], idn[1], z0); VE(3); VA(2); VR(1); VM(0); MG(3); SB();
        z1 = mfma(aa[0], qf[1][0], zz); VE(4); VA(3); VR(2); VM(1); VC(0); MG(4); SB();
      };
      auto partB = [&](int T, auto BC) {
        constexpr int B = decltype(BC)::value;
        u32x4 pn[4], an[2];
        asm volatile("s_waitcnt vmcnt(5)" ::: "memory");
        load_ops(wd1, B, pn, an);
        z1 = mfma(aa[1], qf[1][1], z1); VE(5); VA(4); VR(3); VM(2); VC(1); MG(5); LL(0); LL(1); SB();
        z1 = mfma(pp[2], idn[0], z1); VE(6); VA(5); VR(4); VM(3); VC(2); MG(6); LL(2); LL(3); SB();
        z1 = mfma(pp[3], idn[1], z1); VE(7); VA(6); VR(5); VM(4); VC(3); MG(7); LL(4); LL(5);
        asm volatile("" ::"v"(pn[0]), "v"(pn[1]), "v"(pn[2]), "v"(pn[3]), "v"(an[1]));
        dmaP(B, wd4);
        dmaE(T + 3, B, srdE0);
        SB();
        S[0] = mfma(h0[0], bp[0], S[0]); VA(7); VR(6); VM(5); VC(4); LL(6); LL(7); LL(8); SB();
        S[0] = mfma(h0[1], bp[1], S[0]); VR(7); VM(6); VC(5); LL(9); LL(10); LL(11);
        bn[0] = lds_ld<u32x4>(L_BLUT + mb0 * 16); bn[1] = lds_ld<u32x4>(L_BLUT + mb1 * 16);
        SB();
        S[1] = mfma(h1[0], bp[0], S[1]); VM(7); VC(6); LL(12); LL(13); LL(14); SB();
        VC(7); LL(15);
        S[1] = mfma(h1[1], bp[1], S[1]);
        asm volatile("s_cmp_lg_i32 %0, %1\n\ts_cbranch_scc1 1f\n\ts_setprio 0\n1:" ::"s"(T), "s"(tflip) : "scc");
        SB();
        z1p = z1;
        bp[0] = bn[0];
        bp[1] = bn[1];
        pp[0] = pn[0]; pp[1] = pn[1]; pp[2] = pn[2]; pp[3] = pn[3];
        aa[0] = an[0]; aa[1] = an[1];
      };
      const std::integral_constant<int, 0> B0;
      const std::integral_constant<int, 1> B1;
      partA(0);
      int t = 0;
      for (; t + 2 < ntiles; t += 2) {
        partB(t, B1);
        partA(t + 1);
        partB(t + 1, B0);
        partA(t + 2);
      }
      partB(t, B1);
      if (t + 1 < ntiles) {
        partA(t + 1);
        partB(t + 1, B0);
      }
      load_w2();
#pragma unroll
      for (int j = 0; j < 8; ++j) { VE(j); VA(j); VR(j); VM(j); VC(j); MG(j); }
      S[0] = mfma(h0[0], bp[0], S[0]);
      S[1] = mfma(h1[0], bp[0], S[1]);
      S[0] = mfma(h0[1], bp[1], S[0]);
      S[1] = mfma(h1[1], bp[1], S[1]);
      __builtin_amdgcn_s_setprio(0);
    }
    asm volatile("s_waitcnt vmcnt(0)" ::: "memory");
  } else {
    int round = 0;
    do {
      const unsigned lo = (unsigned)round * CAP;
      const unsigned rem = total > lo ? min(total - lo, (unsigned)CAP) : 0u;
      const __amdgpu_buffer_rsrc_t srdE = __builtin_amdgcn_make_buffer_rsrc((void*)eap, (short)0, (int)(rem * 8), 0x00020000);
      if (round > 0) {
        __syncthreads();
        for (int i = tt; i < 36 * 32; i += 256) lds_st<unsigned>(wbase + i * 4, PADWORD);
        for (int i = tt; i < 34 * 32; i += 256) lds_st<unsigned>(L_MASK + team * MASK_TEAM + i * 4, 0u);
        for (unsigned k = 0; k < segc; ++k) {
          const unsigned p = pos0 + k - lo;
          if (p < (unsigned)CAP) lds_st<unsigned>(IDX + p * 4, rec0 + k);
        }
        __syncthreads();
        fetch_records(rem);
#pragma unroll
        for (int k = 0; k < 4; ++k)
          if (tt + 256u * k < rem) place_at(tt + 256u * k, rec[k]);
        asm volatile("s_waitcnt vmcnt(0)" ::: "memory");
        __syncthreads();
        dmaP(0, lds_ld<u32x4>(w4a));
        dmaE(0, 0, srdE);
        dmaP(1, lds_ld<u32x4>(w4a + 128));
        dmaE(1, 1, srdE);
      }
      const int ntiles = (int)((rem + 31) >> 5);
      if (ntiles > 0) {
        u32x4 pp[4], aa[2];
        asm volatile("s_waitcnt vmcnt(5)" ::: "memory");
        load_ops(lds_ld<unsigned>(w1a), 0, pp, aa);
        {
          const u32x4 wd = lds_ld<u32x4>(w4a + 256);
          KEEP_OPS();
          dmaP(0, wd);
          dmaE(2, 0, srdE);
        }
        for (int t = 0; t < ntiles; ++t) {
          const int B = (t + 1) & 1;
          f32x16 z0 = mfma(aa[0], qf[0][0], zz), z1 = mfma(aa[0], qf[1][0], zz);
          z0 = mfma(aa[1], qf[0][1], z0);
          z1 = mfma(aa[1], qf[1][1], z1);
          z0 = mfma(pp[0], idn[0], z0);
          z1 = mfma(pp[2], idn[0], z1);
          z0 = mfma(pp[1], idn[1], z0);
          z1 = mfma(pp[3], idn[1], z1);
          u32x4 bb[2], h0[2], h1[2];
          bb[0] = lds_ld<u32x4>(L_BLUT + lds_ld<unsigned char>(mka + t * 128) * 16);
          bb[1] = lds_ld<u32x4>(L_BLUT + lds_ld<unsigned char>(mka + t * 128 + 2) * 16);
#pragma unroll
          for (int j = 0; j < 8; ++j) {
            const float za = z0[2 * j] * 64.0f, zb = z0[2 * j + 1] * 64.0f, ya = z1[2 * j], yb = z1[2 * j + 1];
            h0[j >> 2][j & 3] = pack_bf16(za * __builtin_amdgcn_rcpf(1.0f + __builtin_amdgcn_exp2f(-LOG2E * za)), zb * __builtin_amdgcn_rcpf(1.0f + __builtin_amdgcn_exp2f(-LOG2E * zb)));
            h1[j >> 2][j & 3] = pack_bf16(ya * __builtin_amdgcn_rcpf(1.0f + __builtin_amdgcn_exp2f(-ya)), yb * __builtin_amdgcn_rcpf(1.0f + __builtin_amdgcn_exp2f(-yb)));
          }
          S[0] = mfma(h0[0], bb[0], S[0]);
          S[1] = mfma(h1[0], bb[0], S[1]);
          S[0] = mfma(h0[1], bb[1], S[0]);
          S[1] = mfma(h1[1], bb[1], S[1]);
          asm volatile("s_waitcnt vmcnt(5)" ::: "memory");
          const u32x4 wd = lds_ld<u32x4>(w4a + (t + 3) * 128);
          load_ops(lds_ld<unsigned>(w1a + (t + 1) * 128), B, pp, aa);
          KEEP_OPS();
          dmaP(B, wd);
          dmaE(t + 3, B, srdE);
        }
      }
      asm volatile("s_waitcnt vmcnt(0)" ::: "memory");
    } while (++round < nrounds);
  }

  STAMP(16);
  CSTAMP(31);
  if (!w2_loaded) load_w2();
#pragma unroll
  for (int t = 0; t < 2; ++t) {
    const float sc = t ? LN2 : 1.0f;
#pragma unroll
    for (int s = 0; s < 2; ++s) {
      u32x4 v;
#pragma unroll
      for (int j = 0; j < 4; ++j) v[j] = pack_bf16(S[t][8 * s + 2 * j] * sc, S[t][8 * s + 2 * j + 1] * sc);
      lds_st<u32x4>(L_STG + wv * 8192 + (2 * t + s) * 1024 + lane * 16, v);
    }
  }
  __syncthreads();
  STAMP(17);
  const float deg = (float)lds_ld<unsigned>(L_CNT + team * 128 + r * 4);
  f32x16 o, o2;
#pragma unroll
  for (int gq = 0; gq < 4; ++gq) {
    o[4 * gq + 0] = deg * __uint_as_float(b2v[gq][0] << 16);
    o[4 * gq + 1] = deg * __uint_as_float(b2v[gq][0] & 0xFFFF0000u);
    o[4 * gq + 2] = deg * __uint_as_float(b2v[gq][1] << 16);
    o[4 * gq + 3] = deg * __uint_as_float(b2v[gq][1] & 0xFFFF0000u);
  }
#pragma unroll
  for (int i = 0; i < 16; ++i) o2[i] = 0.f;
  const unsigned EX = L_STG + team * 32768 + lane * 16;
#pragma unroll
  for (int ks = 0; ks < 8; ++ks) {
    o = mfma(w2f[ks], lds_ld<u32x4>(EX + (ks >> 2) * 8192 + (ks & 3) * 1024), o);
    o2 = mfma(w2f[8 + ks], lds_ld<u32x4>(EX + ((8 + ks) >> 2) * 8192 + (ks & 3) * 1024), o2);
  }
#pragma unroll
  for (int i = 0; i < 16; ++i) o[i] += o2[i];
  if (r < G) {
    unsigned short* op = out + (size_t)(n0 + r) * 128 + 32 * w + 4 * h;
#pragma unroll
    for (int gq = 0; gq < 4; ++gq) {
      u32x2 v;
      v[0] = pack_bf16(o[4 * gq], o[4 * gq + 1]);
      v[1] = pack_bf16(o[4 * gq + 2], o[4 * gq + 3]);
      *(u32x2*)(op + 8 * gq) = v;
    }
  }
  STAMP(18);
}

}

extern "C" void kernel_launch(void* const* d_in, const int* in_sizes, int n_in, void* d_out, int out_size, void* d_ws,
                              size_t ws_size, hipStream_t stream) {
  const unsigned short* x = (const unsigned short*)d_in[0];
  const uint32_t* ea = (const uint32_t*)d_in[1];
  const int* src = (const int*)d_in[2];
  const int* dst = (const int*)d_in[3];
  const unsigned short* w1 = (const unsigned short*)d_in[4];
  const unsigned short* b1 = (const unsigned short*)d_in[5];
  const unsigned short* w2 = (const unsigned short*)d_in[6];
  const unsigned short* b2 = (const unsigned short*)d_in[7];
  (void)in_sizes; (void)n_in; (void)out_size; (void)ws_size;
  prep_kernel<<<PB_TBL, 512, 0, stream>>>(x, ea, src, dst, w1, w2, (char*)d_ws);
  main_kernel<<<MAIN_GRID, 512, 0, stream>>>(x, w1, b1, b2, (char*)d_ws, (unsigned short*)d_out);
}
```
